```python
import torch

def _softmax(x, axis=-1):
    return torch.softmax(x, dim=axis)
def _gelu(x):
    return torch.nn.functional.gelu(x, approximate="tanh")
def _silu(x):
    return torch.nn.functional.silu(x)
def _sigmoid(x):
    return torch.sigmoid(x)
def _layernorm(x, axis=-1, eps=1e-5):
    m = x.mean(dim=axis, keepdim=True)
    v = x.var(dim=axis, keepdim=True, unbiased=False)
    return (x - m) / torch.sqrt(v + eps)
def _scatter_add(tgt, idx, src, axis=0):
    return tgt.clone().index_add_(axis, idx, src)
def _segment_sum(src, seg, n):
    out = torch.zeros((n,) + src.shape[1:], dtype=src.dtype)
    return out.index_add_(0, seg, src)
def _pad(x, pw):
    flat = tuple(v for pair in reversed(pw) for v in pair)
    return torch.nn.functional.pad(x, flat)

kernel_name = 'gated_mix__sliding_local_dilated__grouped_kv_mqa__prompt'

def setup_inputs():

    _w = (lambda a: (lambda t: t.to(torch.bfloat16) if t.is_floating_point() else t)(torch.as_tensor(a)))
    def _A_setup():
        B, S, D = 1, 2048, 512
        return {
            "x": _w((lambda *s: torch.randn(*s, dtype=torch.bfloat16) * 0.05)(B, S, D)),
            "wqkv": _w((lambda *s: torch.randn(*s, dtype=torch.bfloat16) * 0.8)(D, 3 * D)),
            "wo": _w((lambda *s: torch.randn(*s, dtype=torch.bfloat16) * 0.05)(D, D)),
        }
    _A_in = _A_setup()

    def _B_setup():
        B, S, D, H = 1, 2048, 512, 32
        Dh = D // H
        return {
            "x": _w((lambda *s: torch.randn(*s, dtype=torch.bfloat16) * 0.05)(B, S, D)),
            "wq": _w((lambda *s: torch.randn(*s, dtype=torch.bfloat16) * 1.6)(D, H * Dh)),
            "wk": _w((lambda *s: torch.randn(*s, dtype=torch.bfloat16) * 1.6)(D, Dh)),
            "wv": _w((lambda *s: torch.randn(*s, dtype=torch.bfloat16) * 0.2)(D, Dh)),
            "wo": _w((lambda *s: torch.randn(*s, dtype=torch.bfloat16) * 0.2)(H * Dh, D)),
        }
    _B_in = _B_setup()

    return {
        "A_x": _A_in["x"],
        "A_wqkv": _A_in["wqkv"],
        "A_wo": _A_in["wo"],
        "B_x": _B_in["x"],
        "B_wq": _B_in["wq"],
        "B_wk": _B_in["wk"],
        "B_wv": _B_in["wv"],
        "B_wo": _B_in["wo"],
        "gate_w": _w((lambda *s: torch.randn(*s, dtype=torch.bfloat16) * 0.05)(512) * 16.0),
    }

def reference(A_x, A_wqkv, A_wo, B_x, B_wq, B_wk, B_wv, B_wo, gate_w):
    def _A_ref(x, wqkv, wo):
        B, S, D = x.shape
        H = 32; Dh = D // H
        W, dil = 8, 2
        qkv = (lambda x, *d: x.permute(*d))((x @ wqkv).reshape(B, S, 3, H, Dh), 2, 0, 3, 1, 4)
        q, k, v = qkv[0], qkv[1], qkv[2]
        scale = Dh ** -0.5
        pos = torch.arange(S)
        off = torch.arange(W) * dil
        idx = pos[:, None] - off[None, :]
        mask = idx >= 0
        idx_c = torch.where(mask, idx, 0).reshape(-1)
        kg = k[:, :, idx_c, :].reshape(B, H, S, W, Dh)
        vg = v[:, :, idx_c, :].reshape(B, H, S, W, Dh)
        logits = torch.einsum("bhsd,bhswd->bhsw", q, kg) * scale
        logits = torch.where(mask[None, None, :, :], logits, -1e9)
        attn = _softmax(logits, axis=-1)
        o = torch.einsum("bhsw,bhswd->bhsd", attn, vg)
        return (lambda x, *d: x.permute(*d))(o, 0, 2, 1, 3).reshape(B, S, D) @ wo

    def _B_ref(x, wq, wk, wv, wo):
        B, S, D = x.shape
        H = 32
        Dh = D // H
        q = (lambda x, *d: x.permute(*d))((x @ wq).reshape(B, S, H, Dh), 0, 2, 1, 3)
        k = x @ wk
        v = x @ wv
        scores = torch.einsum("bhqd,bkd->bhqk", q, k) * (Dh ** -0.5)
        p = _softmax(scores, axis=-1)
        o = torch.einsum("bhqk,bkd->bhqd", p, v)
        return (lambda x, *d: x.permute(*d))(o, 0, 2, 1, 3).reshape(B, S, H * Dh) @ wo

    x = A_x
    g = _sigmoid((x * gate_w).sum(-1, keepdims=True))
    a = _A_ref(x, A_wqkv, A_wo)
    b = _B_ref(x, B_wq, B_wk, B_wv, B_wo)
    return g * a + (1 - g) * b
```

```cpp
#include <hip/hip_runtime.h>
#include <hip/hip_bf16.h>

typedef unsigned short u16;
typedef __attribute__((ext_vector_type(4))) float f32x4;
typedef __attribute__((ext_vector_type(16))) float f32x16;
typedef __attribute__((ext_vector_type(8))) __bf16 bf16x8;
typedef __attribute__((ext_vector_type(4))) __bf16 bf16x4;
typedef __attribute__((ext_vector_type(4))) short s16x4;
typedef __attribute__((ext_vector_type(8))) short s16x8;
typedef __attribute__((ext_vector_type(4))) unsigned u32x4;
typedef __attribute__((ext_vector_type(2))) unsigned u32x2;
#define LDS_AS __attribute__((address_space(3)))
typedef LDS_AS const char* lds_cptr;

#define MFMA32(a, b, c) __builtin_amdgcn_mfma_f32_32x32x16_bf16(a, b, c, 0, 0, 0)
#define MFMA16(a, b, c) __builtin_amdgcn_mfma_f32_16x16x32_bf16(a, b, c, 0, 0, 0)
#define MFMA16L(a, b, c) __builtin_amdgcn_mfma_f32_16x16x16bf16_1k(a, b, c, 0, 0, 0)
#define WAIT_BAR(N) asm volatile("s_waitcnt vmcnt(" #N ") lgkmcnt(0)\n\ts_barrier" ::: "memory")

constexpr size_t WS_QB = 0;
constexpr size_t WS_KV = 2u << 20;
constexpr size_t WS_G = (2u << 20) + (128u << 10);
constexpr size_t WS_O = 3u << 20;


constexpr float QSCALE = 0.25f * 1.4426950408889634f;

__device__ __forceinline__ void glds16(const void* g, unsigned lds_base) {
  unsigned sv;
  asm volatile("s_mov_b32 %0, m0\n\ts_mov_b32 m0, %2\n\ts_nop 0\n\tglobal_load_lds_dwordx4 %1, off\n\ts_mov_b32 m0, %0"
               : "=&s"(sv) : "v"(g), "s"(lds_base) : "memory");
}
__device__ __forceinline__ bf16x8 lds_b128(lds_cptr p) { return *(const LDS_AS bf16x8*)p; }
__device__ __forceinline__ s16x4 lds_tr(lds_cptr p) {
  return __builtin_amdgcn_ds_read_tr16_b64_v4i16((LDS_AS s16x4*)p);
}
__device__ __forceinline__ bf16x8 tr_frag(lds_cptr p, int off2) {
  s16x4 a = lds_tr(p), b = lds_tr(p + off2);
  s16x8 r = __builtin_shufflevector(a, b, 0, 1, 2, 3, 4, 5, 6, 7);
  return __builtin_bit_cast(bf16x8, r);
}
__device__ __forceinline__ s16x4 pack4(float a, float b, float c, float d) {
  bf16x4 r; r[0] = (__bf16)a; r[1] = (__bf16)b; r[2] = (__bf16)c; r[3] = (__bf16)d;
  return __builtin_bit_cast(s16x4, r);
}

constexpr int P_XS = 0;
constexpr int P_WB = 81920;
constexpr int P_WKV = P_WB + 3 * 16384;
constexpr int P_GW = P_WKV + 16384;
constexpr int P_GS = P_GW + 1024;
constexpr int P_LDS = P_GS + 256;

__global__ __launch_bounds__(512, 2) void k_proj(const u16* __restrict__ x, const u16* __restrict__ wqkv,
                                                 const u16* __restrict__ wq, const u16* __restrict__ wk,
                                                 const u16* __restrict__ wv, const u16* __restrict__ gw,
                                                 char* __restrict__ ws) {
  __shared__ __attribute__((aligned(1024))) char smem[P_LDS];
  const unsigned sb = (unsigned)(size_t)(LDS_AS char*)smem;
  lds_cptr S = (lds_cptr)smem;
  const int b = blockIdx.x, xcd = b & 7, jj = b >> 3, tb = 4 * xcd + (jj >> 3), hg = jj & 7;
  const int T0 = 64 * tb;
  const int l = threadIdx.x & 63;
  const int w = __builtin_amdgcn_readfirstlane(threadIdx.x >> 6);
#pragma unroll
  for (int i = 0; i < 10; ++i) {
    int r = w + 8 * i, t = T0 - 16 + r;
    t = t < 0 ? r : t;
    glds16(x + (size_t)t * 512 + ((l ^ (r & 15)) * 8), sb + P_XS + r * 1024);
  }
  {
    const u16* wkv = (hg & 1) ? wv : wk;
    glds16(wkv + (2 * w) * 512 + l * 8, sb + P_WKV + (2 * w) * 1024);
    glds16(wkv + (2 * w + 1) * 512 + l * 8, sb + P_WKV + (2 * w + 1) * 1024);
    glds16(gw + l * 8, sb + P_GW);
  }
  const u16* wsrc[2]; int wstr[2];
#pragma unroll
  for (int i = 0; i < 2; ++i) {
    int row = 4 * w + 2 * i + (l >> 5);
    int s = ((row & 3) << 1) | ((row >> 3) & 1);
    int c = (l & 31) ^ (s << 1);
    int seg = c >> 3;
    int ld = seg == 0 ? 512 : 1536;
    const u16* base = seg == 0 ? wq : wqkv + (seg - 1) * 512;
    wsrc[i] = base + (size_t)row * ld + 64 * hg + 8 * (c & 7);
    wstr[i] = 32 * ld;
  }
  auto stage = [&](int c) {
    unsigned dst = sb + P_WB + (c % 3) * 16384 + w * 2048;
    glds16(wsrc[0] + (size_t)c * wstr[0], dst);
    glds16(wsrc[1] + (size_t)c * wstr[1], dst + 1024);
  };
  stage(0); stage(1);
  const int g = l >> 4, q4 = (l & 15) >> 2, p4 = l & 3, h = l >> 5;

  if (w < 4) {
    const int tt = w & 1, hp = w >> 1;
    f32x16 acc = {0}, gacc = {0};
    f32x4 kvacc = {0};
    const int xr = 16 + 32 * tt + (l & 31);
    const int woff = (8 * h + q4) * 512 + (((4 * hp + 2 * (g & 1) + (p4 >> 1)) ^ (((q4 << 1) | h) << 1)) * 16) + (p4 & 1) * 8;
    const int kr = 16 + 16 * (hg >> 1) + (l & 15);
#pragma unroll
    for (int c = 0; c < 16; ++c) {
      if (c < 15) WAIT_BAR(2); else WAIT_BAR(0);
      if (c + 2 < 16) stage(c + 2);
      lds_cptr wb = S + P_WB + (c % 3) * 16384;
#pragma unroll
      for (int ks = 0; ks < 2; ++ks) {
        bf16x8 xf = lds_b128(S + P_XS + xr * 1024 + (((4 * c + 2 * ks + h) ^ (xr & 15)) * 16));
        bf16x8 wf = tr_frag(wb + woff + ks * 8192, 2048);
        acc = MFMA32(wf, xf, acc);
        if (hp == 1) {
          bf16x8 gf = lds_b128(S + P_GW + (2 * c + ks) * 32 + h * 16);
          gacc = MFMA32(gf, xf, gacc);
        }
      }
      if (w == 0) {
        bf16x8 xf = lds_b128(S + P_XS + kr * 1024 + (((4 * c + g) ^ (kr & 15)) * 16));
        bf16x8 wf = tr_frag(S + P_WKV + (32 * c + 8 * g + q4) * 32 + p4 * 8, 128);
        if (hg & 1) kvacc = MFMA16(xf, wf, kvacc); else kvacc = MFMA16(wf, xf, kvacc);
      }
    }
#pragma unroll
    for (int s = 0; s < 2; ++s) {
      bf16x8 qf;
#pragma unroll
      for (int j = 0; j < 8; ++j) qf[j] = (__bf16)(acc[8 * s + j] * QSCALE);
      *(bf16x8*)(ws + WS_QB + ((size_t)(2 * tb + tt) * 32 + 4 * hg + 2 * hp + s) * 1024 + l * 16) = qf;
    }
    if (hp == 1) {
      float gv = 1.f / (1.f + __expf(-gacc[0]));
      if (l < 32) {
        *(LDS_AS float*)(smem + P_GS + (32 * tt + l) * 4) = gv;
        if (hg == 0) *(float*)(ws + WS_G + (size_t)(T0 + 32 * tt + l) * 4) = gv;
      }
    }
    if (w == 0) {
      s16x4 pk = pack4(kvacc[0], kvacc[1], kvacc[2], kvacc[3]);
      int tk = T0 + 16 * (hg >> 1);
      char* tile = ws + WS_KV + (size_t)(tk >> 5) * 2048;
      int sblk = (tk >> 4) & 1;
      if (hg & 1) *(s16x4*)(tile + 1024 + (g >> 1) * 512 + sblk * 256 + (l & 15) * 16 + (g & 1) * 8) = pk;
      else        *(s16x4*)(tile + (g & 1) * 512 + (16 * sblk + (l & 15)) * 16 + (g >> 1) * 8) = pk;
    }
    WAIT_BAR(0);
  } else {
    const int hh = w - 4;
    f32x4 qa[4], ka[5], va[5];
#pragma unroll
    for (int i = 0; i < 5; ++i) { ka[i] = f32x4{0, 0, 0, 0}; va[i] = f32x4{0, 0, 0, 0}; if (i < 4) qa[i] = f32x4{0, 0, 0, 0}; }
    int woff[3];
#pragma unroll
    for (int f = 0; f < 3; ++f)
      woff[f] = (8 * g + q4) * 512 + (((8 * (f + 1) + 2 * hh + (p4 >> 1)) ^ (((q4 << 1) | (g & 1)) << 1)) * 16) + (p4 & 1) * 8;
#pragma unroll
    for (int c = 0; c < 16; ++c) {
      if (c < 15) WAIT_BAR(2); else WAIT_BAR(0);
      if (c + 2 < 16) stage(c + 2);
      lds_cptr wb = S + P_WB + (c % 3) * 16384;
      bf16x8 wqf = tr_frag(wb + woff[0], 2048), wkf = tr_frag(wb + woff[1], 2048), wvf = tr_frag(wb + woff[2], 2048);
#pragma unroll
      for (int i = 0; i < 5; ++i) {
        bf16x8 xf = lds_b128(S + P_XS + (16 * i + (l & 15)) * 1024 + (((4 * c + g) ^ (l & 15)) * 16));
        ka[i] = MFMA16(wkf, xf, ka[i]);
        va[i] = MFMA16(xf, wvf, va[i]);
        if (i > 0) qa[i - 1] = MFMA16(wqf, xf, qa[i - 1]);
      }
    }
    s16x4 kb[5], vb[5];
#pragma unroll
    for (int i = 0; i < 5; ++i) { kb[i] = pack4(ka[i][0], ka[i][1], ka[i][2], ka[i][3]); vb[i] = pack4(va[i][0], va[i][1], va[i][2], va[i][3]); }
    f32x4 mprev, mcur;
#pragma unroll
    for (int r = 0; r < 4; ++r) {
      int dc = (l & 15) - (4 * g + r), dp = dc + 16;
      mcur[r] = (dc >= 0 && !(dc & 1)) ? 0.f : -__builtin_inff();
      mprev[r] = (dp <= 14 && !(dp & 1)) ? 0.f : -__builtin_inff();
    }
    WAIT_BAR(0);
#pragma unroll
    for (int i = 0; i < 4; ++i) {
      s16x4 qb = pack4(qa[i][0] * QSCALE, qa[i][1] * QSCALE, qa[i][2] * QSCALE, qa[i][3] * QSCALE);
      f32x4 mp = mprev;
      if (i == 0 && tb == 0) mp = f32x4{-__builtin_inff(), -__builtin_inff(), -__builtin_inff(), -__builtin_inff()};
      f32x4 sp = MFMA16L(kb[i], qb, mp);
      f32x4 sc = MFMA16L(kb[i + 1], qb, mcur);
      float m = fmaxf(fmaxf(fmaxf(sp[0], sp[1]), fmaxf(sp[2], sp[3])), fmaxf(fmaxf(sc[0], sc[1]), fmaxf(sc[2], sc[3])));
      m = fmaxf(m, __shfl_xor(m, 16)); m = fmaxf(m, __shfl_xor(m, 32));
      float sum = 0;
#pragma unroll
      for (int r = 0; r < 4; ++r) { sp[r] = __builtin_amdgcn_exp2f(sp[r] - m); sc[r] = __builtin_amdgcn_exp2f(sc[r] - m); sum += sp[r] + sc[r]; }
      sum += __shfl_xor(sum, 16); sum += __shfl_xor(sum, 32);
      f32x4 o = MFMA16L(vb[i], pack4(sp[0], sp[1], sp[2], sp[3]), (f32x4{0, 0, 0, 0}));
      o = MFMA16L(vb[i + 1], pack4(sc[0], sc[1], sc[2], sc[3]), o);
      float gv = *(const LDS_AS float*)(S + P_GS + (16 * i + (l & 15)) * 4);
      float f = gv / sum;
      s16x4 ob = pack4(o[0] * f, o[1] * f, o[2] * f, o[3] * f);
      int tok = 16 * (i & 1) + (l & 15);
      *(s16x4*)(ws + WS_O + ((size_t)(2 * tb + (i >> 1)) * 64 + 4 * hg + hh) * 1024 + ((g & 1) * 32 + tok) * 16 + (g >> 1) * 8) = ob;
    }
  }
}

#define STR2(x) #x
#define STR(x) STR2(x)
#define EX(r) "v_exp_f32 v" STR(r) ", v" STR(r) "\n\t"
#define TILE_A(KOFF, VOFF0, VOFF1)                                                   \
  EX(112) EX(113) EX(114)                                                            \
  "v_smfmac_f32_32x32x32_bf16 v[144:159], v[80:83], v[136:143], v84\n\t"             \
  EX(115) EX(116) EX(117) EX(118) EX(119)                                            \
  "s_waitcnt lgkmcnt(2)\n\t"                                                         \
  "v_mfma_f32_32x32x16_bf16 v[128:143], v[68:71], v[64:67], v[96:111]\n\t"           \
  "ds_read_b128 v[68:71], v85 offset:" STR(KOFF) "\n\t"                              \
  EX(120) EX(121) EX(122) EX(123) EX(124)                                            \
  "s_waitcnt lgkmcnt(1)\n\t"                                                         \
  "v_smfmac_f32_32x32x32_bf16 v[144:159], v[72:75], v[112:119], v84\n\t"             \
  "s_mov_b64 exec, %[mask]\n\t"                                                      \
  "ds_read_b128 v[72:75], v85 offset:" STR(VOFF0) "\n\t"                             \
  "ds_read_b128 v[80:83], v85 offset:" STR(VOFF1) "\n\t"                             \
  "s_mov_b64 exec, -1\n\t"                                                           \
  EX(125) EX(126) EX(127)
#define TILE_B(KOFF, VOFF0, VOFF1)                                                   \
  EX(128) EX(129) EX(130)                                                            \
  "v_smfmac_f32_32x32x32_bf16 v[144:159], v[76:79], v[120:127], v84\n\t"             \
  EX(131) EX(132) EX(133) EX(134) EX(135)                                            \
  "s_waitcnt lgkmcnt(2)\n\t"                                                         \
  "v_mfma_f32_32x32x16_bf16 v[112:127], v[68:71], v[64:67], v[96:111]\n\t"           \
  "ds_read_b128 v[68:71], v85 offset:" STR(KOFF) "\n\t"                              \
  EX(136) EX(137) EX(138) EX(139) EX(140)                                            \
  "s_waitcnt lgkmcnt(1)\n\t"                                                         \
  "v_smfmac_f32_32x32x32_bf16 v[144:159], v[72:75], v[128:135], v84\n\t"             \
  "s_mov_b64 exec, %[mask]\n\t"                                                      \
  "ds_read_b128 v[72:75], v85 offset:" STR(VOFF0) "\n\t"                             \
  "ds_read_b128 v[76:79], v85 offset:" STR(VOFF1) "\n\t"                             \
  "s_mov_b64 exec, -1\n\t"                                                           \
  EX(141) EX(142) EX(143)
#define MOVC(r) "v_mov_b32 v" STR(r) ", %[negm]\n\t"
#define MOVZ(r) "v_mov_b32 v" STR(r) ", 0\n\t"
#define MOV1(r) "v_mov_b32 v" STR(r) ", 0x3f803f80\n\t"
#define CLOB16(a) "v" STR(a##0), "v" STR(a##1), "v" STR(a##2), "v" STR(a##3), "v" STR(a##4), "v" STR(a##5), "v" STR(a##6), "v" STR(a##7), "v" STR(a##8), "v" STR(a##9)

__device__ __forceinline__ void attn_loop(u32x4 q, float negm, unsigned addr, float (&o)[9]) {
  unsigned long long mask = 0x0000FFFF0000FFFFull;
  unsigned cnt;
  asm volatile(
      "v_mov_b32 v64, %[q0]\n\tv_mov_b32 v65, %[q1]\n\tv_mov_b32 v66, %[q2]\n\tv_mov_b32 v67, %[q3]\n\t"
      "v_mov_b32 v85, %[addr]\n\tv_mov_b32 v84, 0xdddd\n\t"
      "ds_read_b128 v[68:71], v85\n\t"
      MOV1(72) MOV1(73) MOV1(74) MOV1(75) MOV1(76) MOV1(77) MOV1(78) MOV1(79) MOV1(80) MOV1(81) MOV1(82) MOV1(83)
      MOVC(96) MOVC(97) MOVC(98) MOVC(99) MOVC(100) MOVC(101) MOVC(102) MOVC(103)
      MOVC(104) MOVC(105) MOVC(106) MOVC(107) MOVC(108) MOVC(109) MOVC(110) MOVC(111)
      MOVZ(136) MOVZ(137) MOVZ(138) MOVZ(139) MOVZ(140) MOVZ(141) MOVZ(142) MOVZ(143)
      MOVZ(144) MOVZ(145) MOVZ(146) MOVZ(147) MOVZ(148) MOVZ(149) MOVZ(150) MOVZ(151)
      MOVZ(152) MOVZ(153) MOVZ(154) MOVZ(155) MOVZ(156) MOVZ(157) MOVZ(158) MOVZ(159)
      "s_waitcnt lgkmcnt(0)\n\t"
      "v_mfma_f32_32x32x16_bf16 v[112:127], v[68:71], v[64:67], v[96:111]\n\t"
      "ds_read_b128 v[68:71], v85 offset:2048\n\t"
      "s_mov_b64 exec, %[mask]\n\t"
      "ds_read_b128 v[72:75], v85 offset:1024\n\t"
      "ds_read_b128 v[76:79], v85 offset:1280\n\t"
      "s_mov_b64 exec, -1\n\t"
      "s_mov_b32 %[cnt], 16\n\t"
      "s_nop 7\n\t"
      "LOOP%=:\n\t"
      TILE_A(4096, 3072, 3328)
      TILE_B(6144, 5120, 5376)
      TILE_A(8192, 7168, 7424)
      TILE_B(10240, 9216, 9472)
      "v_add_u32 v85, 0x2000, v85\n\t"
      "s_sub_u32 %[cnt], %[cnt], 1\n\t"
      "s_cmp_lg_u32 %[cnt], 0\n\t"
      "s_cbranch_scc1 LOOP%=\n\t"
      "v_smfmac_f32_32x32x32_bf16 v[144:159], v[80:83], v[136:143], v84\n\t"
      "s_waitcnt lgkmcnt(0)\n\t"
      "s_nop 15\n\ts_nop 7\n\t"
      "v_mov_b32 %[o0], v144\n\tv_mov_b32 %[o1], v145\n\tv_mov_b32 %[o2], v146\n\tv_mov_b32 %[o3], v147\n\t"
      "v_mov_b32 %[o4], v148\n\tv_mov_b32 %[o5], v149\n\tv_mov_b32 %[o6], v150\n\tv_mov_b32 %[o7], v151\n\t"
      "v_mov_b32 %[o8], v152\n\t"
      : [o0] "=v"(o[0]), [o1] "=v"(o[1]), [o2] "=v"(o[2]), [o3] "=v"(o[3]), [o4] "=v"(o[4]), [o5] "=v"(o[5]),
        [o6] "=v"(o[6]), [o7] "=v"(o[7]), [o8] "=v"(o[8]), [cnt] "=&s"(cnt)
      : [q0] "v"(q[0]), [q1] "v"(q[1]), [q2] "v"(q[2]), [q3] "v"(q[3]), [negm] "v"(negm), [addr] "v"(addr), [mask] "s"(mask)
      : "memory", "scc", "v64", "v65", "v66", "v67", "v68", "v69", CLOB16(7), CLOB16(8), CLOB16(9), CLOB16(10), CLOB16(11),
        CLOB16(12), CLOB16(13), CLOB16(14), CLOB16(15));
}

__device__ __forceinline__ float max16(const f32x16& s) {
  float a = fmaxf(fmaxf(s[0], s[1]), s[2]);
#pragma unroll
  for (int i = 3; i < 15; i += 2) a = fmaxf(fmaxf(a, s[i]), s[i + 1]);
  return fmaxf(a, s[15]);
}

constexpr int A_LDS = 131072 + 4096;
__global__ __launch_bounds__(512, 2) void k_attn(char* __restrict__ ws) {
  __shared__ __attribute__((aligned(1024))) char smem[A_LDS];
  const unsigned sb = (unsigned)(size_t)(LDS_AS char*)smem;
  lds_cptr S = (lds_cptr)smem;
  const int b = blockIdx.x, xcd = b & 7, jj = b >> 3, tb = 4 * xcd + (jj >> 3), hg = jj & 7;
  const int l = threadIdx.x & 63;
  const int w = __builtin_amdgcn_readfirstlane(threadIdx.x >> 6);
  const int tt = 2 * tb + (w >> 2), head = 4 * hg + (w & 3);
#pragma unroll
  for (int i = 0; i < 16; ++i) glds16(ws + WS_KV + (i * 8 + w) * 1024 + l * 16, sb + (i * 8 + w) * 1024);
  u32x4 q = *(const u32x4*)(ws + WS_QB + ((size_t)tt * 32 + head) * 1024 + l * 16);
  float gv = *(const float*)(ws + WS_G + (size_t)(32 * tt + (l & 31)) * 4);
  WAIT_BAR(0);
  bf16x8 qf = __builtin_bit_cast(bf16x8, q);
  f32x16 z = {0};
  float m = max16(MFMA32(lds_b128(S + l * 16), qf, z));
  m = fmaxf(m, __shfl_xor(m, 32));
  float o[9];
  attn_loop(q, -m, sb + l * 16, o);
  {
    bool ok = o[8] < 1.2676506e30f;
#pragma unroll
    for (int i = 0; i < 8; ++i) ok = ok && (fabsf(o[i]) < 3.0e38f);
    if (__builtin_amdgcn_ballot_w64(!ok) != 0) {
      for (int t = 1; t < 64; ++t) m = fmaxf(m, max16(MFMA32(lds_b128(S + t * 2048 + l * 16), qf, z)));
      m = fmaxf(m, __shfl_xor(m, 32));
      attn_loop(q, -m, sb + l * 16, o);
    }
  }
  float f = (1.f - gv) / o[8];
  bf16x8 ob;
#pragma unroll
  for (int j = 0; j < 8; ++j) ob[j] = (__bf16)(o[j] * f);
  *(bf16x8*)(ws + WS_O + ((size_t)tt * 64 + 32 + head) * 1024 + l * 16) = ob;
}

constexpr int O_LDS = 131072;
__global__ __launch_bounds__(512, 2) void k_out(const u16* __restrict__ woa, const u16* __restrict__ wob,
                                                const char* __restrict__ ws, u16* __restrict__ out) {
  __shared__ __attribute__((aligned(1024))) char smem[O_LDS];
  const unsigned sb = (unsigned)(size_t)(LDS_AS char*)smem;
  lds_cptr S = (lds_cptr)smem;
  const int b = blockIdx.x, xcd = b & 7, jj = b >> 3, tb = 4 * xcd + (jj >> 3), cg = jj & 7;
  const int l = threadIdx.x & 63;
  const int w = __builtin_amdgcn_readfirstlane(threadIdx.x >> 6);
  const int tt = w & 1, ct = (w >> 1) & 1, kh = w >> 2;
  {
    const u16* wsrc = kh ? wob : woa;
    int row = l >> 3, c = (l & 7) ^ (((row >> 1) & 1) << 2);
    const u16* p = wsrc + (size_t)((w & 3) * 8 + row) * 512 + 64 * cg + 8 * c;
#pragma unroll
    for (int i = 0; i < 16; ++i) glds16(p + (size_t)i * 32 * 512, sb + kh * 65536 + (i * 4 + (w & 3)) * 1024);
  }
  bf16x8 of[32];
#pragma unroll
  for (int i = 0; i < 32; ++i) of[i] = *(const bf16x8*)(ws + WS_O + ((size_t)(2 * tb + tt) * 64 + 32 * kh + i) * 1024 + l * 16);
  WAIT_BAR(0);
  const int g = l >> 4, q4 = (l & 15) >> 2, p4 = l & 3, h = l >> 5;
  const int woff = kh * 65536 + (4 * h + q4) * 128 + (((4 * ct + 2 * (g & 1) + (p4 >> 1)) ^ (((q4 >> 1) & 1) << 2)) * 16) + (p4 & 1) * 8;
  f32x16 acc = {0};
#pragma unroll
  for (int i = 0; i < 32; ++i) acc = MFMA32(tr_frag(S + woff + i * 2048, 1024), of[i], acc);
  WAIT_BAR(0);
  if (kh == 1) {
#pragma unroll
    for (int i = 0; i < 4; ++i)
      *(LDS_AS f32x4*)(smem + (w & 3) * 4096 + i * 1024 + l * 16) = f32x4{acc[4 * i], acc[4 * i + 1], acc[4 * i + 2], acc[4 * i + 3]};
  }
  WAIT_BAR(0);
  if (kh == 0) {
    u16* orow = out + (size_t)(64 * tb + 32 * tt + (l & 31)) * 512 + 64 * cg + 32 * ct + 4 * h;
#pragma unroll
    for (int i = 0; i < 4; ++i) {
      f32x4 v = *(const LDS_AS f32x4*)(S + (w & 3) * 4096 + i * 1024 + l * 16);
      *(s16x4*)(orow + 8 * i) = pack4(acc[4 * i] + v[0], acc[4 * i + 1] + v[1], acc[4 * i + 2] + v[2], acc[4 * i + 3] + v[3]);
    }
  }
}

extern "C" void kernel_launch(void* const* d_in, const int* in_sizes, int n_in, void* d_out, int out_size, void* d_ws,
                              size_t ws_size, hipStream_t stream) {
  const u16* x = (const u16*)d_in[0];
  k_proj<<<256, 512, 0, stream>>>(x, (const u16*)d_in[1], (const u16*)d_in[4], (const u16*)d_in[5], (const u16*)d_in[6],
                                  (const u16*)d_in[8], (char*)d_ws);
  k_attn<<<256, 512, 0, stream>>>((char*)d_ws);
  k_out<<<256, 512, 0, stream>>>((const u16*)d_in[2], (const u16*)d_in[7], (const char*)d_ws, (u16*)d_out);
}
```

```cpp
#include <hip/hip_runtime.h>
#include <hip/hip_bf16.h>

typedef unsigned short u16;
typedef unsigned long long u64;
typedef __attribute__((ext_vector_type(4))) float f32x4;
typedef __attribute__((ext_vector_type(16))) float f32x16;
typedef __attribute__((ext_vector_type(32))) float f32x32;
typedef __attribute__((ext_vector_type(8))) __bf16 bf16x8;
typedef __attribute__((ext_vector_type(4))) __bf16 bf16x4;
typedef __attribute__((ext_vector_type(4))) short s16x4;
typedef __attribute__((ext_vector_type(8))) short s16x8;
typedef __attribute__((ext_vector_type(4))) unsigned u32x4;
typedef __attribute__((ext_vector_type(2))) unsigned u32x2;
typedef __attribute__((ext_vector_type(32))) unsigned u32x32;
#define LDS_AS __attribute__((address_space(3)))
typedef LDS_AS const char* lds_cptr;
typedef LDS_AS char* lds_ptr;

#define MFMA32(a, b, c) __builtin_amdgcn_mfma_f32_32x32x16_bf16(a, b, c, 0, 0, 0)
#define MFMA32K8(a, b, c) __builtin_amdgcn_mfma_f32_32x32x8bf16_1k(a, b, c, 0, 0, 0)
#define MFMA16(a, b, c) __builtin_amdgcn_mfma_f32_16x16x32_bf16(a, b, c, 0, 0, 0)
#define WAIT_BAR(N) asm volatile("s_waitcnt vmcnt(" #N ") lgkmcnt(0)\n\ts_barrier" ::: "memory")
#define LBAR() asm volatile("s_waitcnt lgkmcnt(0)\n\ts_barrier" ::: "memory")
#define PIN() __builtin_amdgcn_sched_barrier(0)

constexpr unsigned WS_EPOCH = 0;
constexpr unsigned WS_FKV = 4096;
constexpr unsigned WS_FA = 8192;
constexpr unsigned WS_FB = 12288;
constexpr unsigned WS_G = 16384;
constexpr unsigned WS_KV = 65536;
constexpr unsigned WS_O = 1u << 20;


constexpr float QSCALE = 0.25f * 1.4426950408889634f;

__device__ __forceinline__ void dma16(unsigned voff, const void* sbase, unsigned lds) {
  unsigned sv;
  asm volatile("s_mov_b32 %0, m0\n\ts_mov_b32 m0, %3\n\ts_nop 0\n\tglobal_load_lds_dwordx4 %1, %2\n\ts_mov_b32 m0, %0"
               : "=&s"(sv) : "v"(voff), "s"(sbase), "s"(lds) : "memory");
}
__device__ __forceinline__ u32x4 gld16(unsigned voff, const void* sbase) {
  u32x4 r;
  asm volatile("global_load_dwordx4 %0, %1, %2" : "=v"(r) : "v"(voff), "s"(sbase) : "memory");
  return r;
}
__device__ __forceinline__ u32x4 gld16_sc1(unsigned voff, const void* sbase) {
  u32x4 r;
  asm volatile("global_load_dwordx4 %0, %1, %2 sc1" : "=v"(r) : "v"(voff), "s"(sbase) : "memory");
  return r;
}
__device__ __forceinline__ void gst16_sc1(unsigned voff, void* sbase, u32x4 v) {
  asm volatile("global_store_dwordx4 %0, %1, %2 sc1\n\ts_nop 1" :: "v"(voff), "v"(v), "s"(sbase) : "memory");
}
__device__ __forceinline__ void gst8_sc1(unsigned voff, void* sbase, u32x2 v) {
  asm volatile("global_store_dwordx2 %0, %1, %2 sc1\n\ts_nop 1" :: "v"(voff), "v"(v), "s"(sbase) : "memory");
}
__device__ __forceinline__ void gst4_sc1(unsigned voff, void* sbase, unsigned v) {
  asm volatile("global_store_dword %0, %1, %2 sc1\n\ts_nop 1" :: "v"(voff), "v"(v), "s"(sbase) : "memory");
}
__device__ __forceinline__ void poll_flags(unsigned voff, const void* sbase, unsigned tlo, unsigned thi) {
  for (int it = 0; it < (1 << 20); ++it) {
    u32x2 f;
    asm volatile("global_load_dwordx2 %0, %1, %2 sc1\n\ts_waitcnt vmcnt(0)" : "=&v"(f) : "v"(voff), "s"(sbase) : "memory");
    if (__builtin_amdgcn_ballot_w64(f[0] != tlo || f[1] != thi) == 0) break;
    __builtin_amdgcn_s_sleep(2);
  }
}
__device__ __forceinline__ bf16x8 lds_b128(lds_cptr p) { return *(const LDS_AS bf16x8*)p; }
__device__ __forceinline__ s16x4 lds_tr(lds_cptr p) {
  return __builtin_amdgcn_ds_read_tr16_b64_v4i16((LDS_AS s16x4*)p);
}
__device__ __forceinline__ bf16x8 tr_frag(lds_cptr p, int off2) {
  s16x4 a = lds_tr(p), b = lds_tr(p + off2);
  s16x8 r = __builtin_shufflevector(a, b, 0, 1, 2, 3, 4, 5, 6, 7);
  return __builtin_bit_cast(bf16x8, r);
}
__device__ __forceinline__ s16x4 pack4(float a, float b, float c, float d) {
  bf16x4 r; r[0] = (__bf16)a; r[1] = (__bf16)b; r[2] = (__bf16)c; r[3] = (__bf16)d;
  return __builtin_bit_cast(s16x4, r);
}
template <int O> __device__ __forceinline__ bf16x8 pack8(const f32x16& a, float s) {
  bf16x8 r;
#pragma unroll
  for (int j = 0; j < 8; ++j) r[j] = (__bf16)(a[O + j] * s);
  return r;
}
__device__ __forceinline__ float xhalf_max(float v) {
  auto r = __builtin_amdgcn_permlane32_swap(__float_as_uint(v), __float_as_uint(v), false, false);
  return fmaxf(__uint_as_float(r[0]), __uint_as_float(r[1]));
}
__device__ __forceinline__ float max16(const f32x16& s) {
  float a = fmaxf(fmaxf(s[0], s[1]), s[2]);
#pragma unroll
  for (int i = 3; i < 15; i += 2) a = fmaxf(fmaxf(a, s[i]), s[i + 1]);
  return fmaxf(a, s[15]);
}

#define STR2(x) #x
#define STR(x) STR2(x)
#define EX(r) "v_exp_f32 v" STR(r) ", v" STR(r) "\n\t"
#define SM(O, V, S) "v_smfmac_f32_32x32x32_bf16 " O ", " V ", " S ", v84\n\t"
#define MF(S, Q, C) "v_mfma_f32_32x32x16_bf16 " S ", v[72:75], " Q ", " C "\n\t"
#define T64(V1P, V1C, KOFF, VOFF0, VOFF1) \
  EX(128) EX(129) EX(130) \
  SM("v[176:191]", V1P, "v[152:159]") \
  EX(131) EX(132) EX(133) EX(134) EX(135) \
  MF("v[144:159]", "v[68:71]", "v[112:127]") \
  "ds_read_b128 v[72:75], v85 offset:" STR(KOFF) "\n\t" \
  EX(136) EX(137) EX(138) EX(139) EX(140) \
  "s_waitcnt lgkmcnt(1)\n\t" \
  SM("v[160:175]", "v[76:79]", "v[128:135]") \
  EX(141) EX(142) EX(143) \
  EX(144) EX(145) EX(146) \
  SM("v[160:175]", V1C, "v[136:143]") \
  EX(147) EX(148) EX(149) EX(150) EX(151) \
  "s_waitcnt lgkmcnt(0)\n\t" \
  MF("v[128:143]", "v[64:67]", "v[96:111]") \
  EX(152) EX(153) EX(154) EX(155) EX(156) \
  SM("v[176:191]", "v[76:79]", "v[144:151]") \
  "s_mov_b64 exec, %[mask]\n\t" \
  "ds_read_b128 v[76:79], v85 offset:" STR(VOFF0) "\n\t" \
  "ds_read_b128 " V1P ", v85 offset:" STR(VOFF1) "\n\t" \
  "s_mov_b64 exec, -1\n\t" \
  EX(157) EX(158) EX(159)
#define ATT_OPS "+{v[64:95]}"(st), "+{v[96:127]}"(cc), "+{v[128:159]}"(ss), "+{v[160:191]}"(oo)
constexpr u64 VMASK = 0x0000FFFF0000FFFFull;

__device__ __forceinline__ void att_pre(u32x32& st, f32x32& cc, f32x32& ss, f32x32& oo) {
  asm volatile(
      "ds_read_b128 v[72:75], v85\n\t"
      "s_mov_b64 exec, %[mask]\n\t"
      "ds_read_b128 v[76:79], v85 offset:1024\n\t"
      "ds_read_b128 v[80:83], v85 offset:1280\n\t"
      "s_mov_b64 exec, -1\n\t"
      "s_waitcnt lgkmcnt(0)\n\t"
      MF("v[128:143]", "v[64:67]", "v[96:111]")
      "s_nop 15\n\t"
      : ATT_OPS : [mask] "s"(VMASK) : "memory");
}
__device__ __forceinline__ void att_seg(u32x32& st, f32x32& cc, f32x32& ss, f32x32& oo, unsigned n4) {
  asm volatile(
      "LOOP%=:\n\t"
      T64("v[88:91]", "v[80:83]", 2048, 3072, 3328)
      T64("v[80:83]", "v[88:91]", 4096, 5120, 5376)
      T64("v[88:91]", "v[80:83]", 6144, 7168, 7424)
      T64("v[80:83]", "v[88:91]", 8192, 9216, 9472)
      "v_add_u32 v85, 0x2000, v85\n\t"
      "s_sub_u32 %[cnt], %[cnt], 1\n\t"
      "s_cmp_lg_u32 %[cnt], 0\n\t"
      "s_cbranch_scc1 LOOP%=\n\t"
      "s_waitcnt lgkmcnt(0)\n\t"
      : ATT_OPS, [cnt] "+s"(n4) : [mask] "s"(VMASK) : "memory", "scc");
}
__device__ __forceinline__ void att_post(u32x32& st, f32x32& cc, f32x32& ss, f32x32& oo) {
  asm volatile(
      SM("v[176:191]", "v[88:91]", "v[152:159]")
      "s_nop 15\n\ts_nop 7\n\t"
      : ATT_OPS :: "memory");
}

constexpr int L_XM = 0, L_XH = 65536, L_WR = 81920, L_WKV = 49152;
constexpr int L_EX = 122880;
constexpr int L_MG = 131072;

template <int ROLE>
__device__ __forceinline__ void proj_step(lds_cptr S, int wbase, int xbase, int hbase, int par, f32x16 (&acc)[3]) {
#pragma unroll
  for (int ks = 0; ks < 2; ++ks) {
    bf16x8 wf = tr_frag(S + wbase + ks * 2048, 512);
    int xo = (2 * par + ks) << 5;
    bf16x8 x0 = lds_b128(S + (xbase ^ xo)), x1 = lds_b128(S + (xbase ^ xo) + 4096);
    if (ROLE == 2) { acc[0] = MFMA32(x0, wf, acc[0]); acc[1] = MFMA32(x1, wf, acc[1]); }
    else { acc[0] = MFMA32(wf, x0, acc[0]); acc[1] = MFMA32(wf, x1, acc[1]); }
    if (ROLE != 0) {
      bf16x8 xh = lds_b128(S + (hbase ^ xo));
      if (ROLE == 2) acc[2] = MFMA32(xh, wf, acc[2]); else acc[2] = MFMA32(wf, xh, acc[2]);
    }
  }
}

__global__ __launch_bounds__(512, 2) void k_fused(const u16* __restrict__ x, const u16* __restrict__ wqkv,
                                                  const u16* __restrict__ woa, const u16* __restrict__ wq,
                                                  const u16* __restrict__ wk, const u16* __restrict__ wv,
                                                  const u16* __restrict__ wob, const u16* __restrict__ gw,
                                                  char* ws, u16* __restrict__ out) {
  __shared__ __attribute__((aligned(1024))) char smem[163840];
  const unsigned sb = (unsigned)(size_t)(LDS_AS char*)smem;
  lds_cptr S = (lds_cptr)smem;
  lds_ptr SW = (lds_ptr)smem;
  const int b = blockIdx.x, xcd = b & 7, jj = b >> 3;
  const int tb = 8 * (xcd >> 1) + (jj >> 2), hg = 4 * (xcd & 1) + (jj & 3);
  const int rot = (tb + 2 * (jj & 3)) & 7;
  const int T0 = 64 * tb;
  const int l = threadIdx.x & 63;
  const int w = __builtin_amdgcn_readfirstlane(threadIdx.x >> 6);
  const int g = l >> 4, q4 = (l & 15) >> 2, p4 = l & 3, h = l >> 5, n = l & 31;

  u32x2 ep;
  asm volatile("global_load_dwordx2 %0, %1, %2 sc1" : "=v"(ep) : "v"(0u), "s"(ws + WS_EPOCH) : "memory");
  const unsigned kxo = (unsigned)((T0 + 16 * (hg >> 1) + (l & 15)) * 1024 + 128 * w + 16 * g);
  u32x4 kx0 = gld16(kxo, x), kx1 = gld16(kxo + 64, x);
  u32x4 gw0 = gld16(128 * w + 16 * g, gw), gw1 = gld16(128 * w + 64 + 16 * g, gw);
  {
    const char* wkv = (const char*)((hg & 1) ? wv : wk) + 2048 * w;
    dma16(l * 16, wkv, sb + L_WKV + 2048 * w);
    dma16(l * 16, wkv + 1024, sb + L_WKV + 2048 * w + 1024);
  }
  const int seg = w >> 1, half = w & 1;
  const int ld2 = (w == 0) ? 1024 : 3072;
  const char* wsrc = (w == 0 ? (const char*)wq : (const char*)wqkv + (w - 1) * 1024) + 128 * hg;
  const unsigned wvo = (l >> 3) * ld2 + (((l & 7) ^ (((l >> 4) & 1) << 2)) * 16);
  unsigned xvo[2];
#pragma unroll
  for (int i = 0; i < 2; ++i) {
    int r = 8 * (2 * (w & 3) + i) + (l >> 3);
    int t = T0 + 2 * (r & 31) + (r >> 5);
    xvo[i] = t * 1024 + (((l & 7) ^ ((r >> 1) & 7)) * 16);
  }
  auto stage_w = [&](int c, int slot) {
    int cc = (c + 2 * rot) & 15;
    const char* p = wsrc + (size_t)cc * 32 * ld2;
#pragma unroll
    for (int i = 0; i < 4; ++i) dma16(wvo, p + i * 8 * ld2, sb + L_WR + slot * 16384 + w * 4096 + i * 1024);
  };
  auto stage_x = [&](int j) {
    const char* p = (const char*)x + ((j + rot) & 7) * 128;
    dma16(xvo[0], p, sb + L_XM + j * 8192 + w * 2048);
    dma16(xvo[1], p, sb + L_XM + j * 8192 + w * 2048 + 1024);
  };
  if (w < 4) {
    stage_x(0); stage_w(0, 0); stage_w(1, 1); stage_x(1); stage_w(2, 2); stage_w(3, 3); stage_x(2); stage_w(4, 4);
    asm volatile("s_waitcnt vmcnt(26)" ::: "memory");
  } else {
#pragma unroll
    for (int i = 0; i < 2; ++i) {
      int r = 8 * i + (l >> 3);
      int t = T0 - 16 + 2 * (r & 7) + (r >> 3);
      t = t < 0 ? 0 : t;
      unsigned vo = t * 1024 + (((l & 7) ^ ((r >> 1) & 7)) * 16);
#pragma unroll
      for (int jx = 0; jx < 2; ++jx) {
        int j = 2 * (w - 4) + jx;
        dma16(vo, (const char*)x + ((j + rot) & 7) * 128, sb + L_XH + j * 2048 + i * 1024);
      }
    }
    asm volatile("s_waitcnt vmcnt(4)" ::: "memory");
  }
  asm volatile("" : "+v"(ep), "+v"(kx0), "+v"(kx1), "+v"(gw0), "+v"(gw1) :: "memory");
  const u64 tag = (((u64)__builtin_amdgcn_readfirstlane(ep[1]) << 32) | __builtin_amdgcn_readfirstlane(ep[0])) * 6364136223846793005ull + 1442695040888963407ull;
  const unsigned tlo = (unsigned)tag, thi = (unsigned)(tag >> 32);
  {
    lds_cptr wp = S + L_WKV + 2048 * w + (8 * g + q4) * 32 + p4 * 8;
    bf16x8 w0 = tr_frag(wp, 128), w1 = tr_frag(wp + 1024, 128);
    bf16x8 a0 = __builtin_bit_cast(bf16x8, kx0), a1 = __builtin_bit_cast(bf16x8, kx1);
    f32x4 z = {0, 0, 0, 0}, d;
    if (hg & 1) d = MFMA16(a1, w1, MFMA16(a0, w0, z)); else d = MFMA16(w1, a1, MFMA16(w0, a0, z));
    f32x4 dg = MFMA16(__builtin_bit_cast(bf16x8, gw1), a1, MFMA16(__builtin_bit_cast(bf16x8, gw0), a0, z));
    *(LDS_AS f32x4*)(SW + L_WKV + 2048 * w + l * 16) = d;
    *(LDS_AS float*)(SW + L_WKV + 2048 * w + 1024 + l * 4) = dg[0];
  }
  f32x16 acc[3];
  acc[0] = f32x16{0}; acc[1] = f32x16{0}; acc[2] = f32x16{0};
  const int woff = L_WR + seg * 4096 + (8 * h + q4) * 128 + (((4 * half + 2 * (g & 1) + (p4 >> 1)) ^ (((q4 >> 1) & 1) << 2)) * 16) + (p4 & 1) * 8;
  const int xoff = L_XM + n * 128 + ((h ^ ((n >> 1) & 7)) * 16);
  const int hoff = L_XH + (n & 15) * 128 + ((h ^ ((n >> 1) & 7)) * 16);
  if (w < 4) {
    int slot = 0;
    for (int j = 0; j < 8; ++j) {
      if (j == 0) WAIT_BAR(20); else if (j < 7) WAIT_BAR(14); else WAIT_BAR(4);
      int prev = slot == 0 ? 4 : slot - 1;
      if (j > 0 && j < 6) { stage_x(j + 2); stage_w(2 * j + 4, prev); }
      proj_step<0>(S, woff + slot * 16384, xoff + j * 8192, 0, 0, acc);
      prev = slot; slot = slot == 4 ? 0 : slot + 1;
      if (j < 6) WAIT_BAR(16); else if (j == 6) WAIT_BAR(10); else WAIT_BAR(0);
      if (j < 6) stage_w(2 * j + 5, prev);
      proj_step<0>(S, woff + slot * 16384, xoff + j * 8192, 0, 1, acc);
      slot = slot == 4 ? 0 : slot + 1;
    }
  } else {
    WAIT_BAR(0);
    if (w == 7) {
      f32x4 d = *(const LDS_AS f32x4*)(S + L_WKV + l * 16);
      float dg = *(const LDS_AS float*)(S + L_WKV + 1024 + l * 4);
#pragma unroll
      for (int i = 1; i < 8; ++i) { d += *(const LDS_AS f32x4*)(S + L_WKV + 2048 * i + l * 16); dg += *(const LDS_AS float*)(S + L_WKV + 2048 * i + 1024 + l * 4); }
      u32x2 pk = __builtin_bit_cast(u32x2, pack4(d[0], d[1], d[2], d[3]));
      int tk = T0 + 16 * (hg >> 1), sblk = (hg >> 1) & 1;
      unsigned o = WS_KV + (tk >> 5) * 2048 + ((hg & 1) ? 1024 + (g >> 1) * 512 + sblk * 256 + (l & 15) * 16 + (g & 1) * 8
                                                        : (g & 1) * 512 + (16 * sblk + (l & 15)) * 16 + (g >> 1) * 8);
      gst8_sc1(o, ws, pk);
      if ((hg & 1) && l < 16) gst4_sc1(WS_G + (tk + l) * 4, ws, __float_as_uint(1.f / (1.f + __expf(-dg))));
    }
    int slot = 0;
    for (int j = 0; j < 8; ++j) {
      if (j > 0) LBAR();
      if (w < 6) proj_step<1>(S, woff + slot * 16384, xoff + j * 8192, hoff + j * 2048, 0, acc);
      else proj_step<2>(S, woff + slot * 16384, xoff + j * 8192, hoff + j * 2048, 0, acc);
      slot = slot == 4 ? 0 : slot + 1;
      if (j == 1 && w == 7) {
        asm volatile("s_waitcnt vmcnt(0)" ::: "memory");
        if (l == 0) gst8_sc1(WS_FKV + (tb * 8 + hg) * 8, ws, u32x2{tlo, thi});
      }
      LBAR();
      if (w < 6) proj_step<1>(S, woff + slot * 16384, xoff + j * 8192, hoff + j * 2048, 1, acc);
      else proj_step<2>(S, woff + slot * 16384, xoff + j * 8192, hoff + j * 2048, 1, acc);
      slot = slot == 4 ? 0 : slot + 1;
    }
  }
  poll_flags(WS_FKV + (w * 32 + n) * 8, ws, tlo, thi);
  u32x4 kv[16];
#pragma unroll
  for (int i = 0; i < 16; ++i) kv[i] = gld16_sc1(l * 16 + (i & 3) * 1024, ws + WS_KV + w * 16384 + (i >> 2) * 4096);
  LBAR();
  {
    float sc = w < 4 ? QSCALE : 1.f;
    lds_ptr e = SW + L_EX + (seg * 8 + half * 4) * 1024 + l * 16;
    *(LDS_AS bf16x8*)(e) = pack8<0>(acc[0], sc);
    *(LDS_AS bf16x8*)(e + 1024) = pack8<8>(acc[0], sc);
    *(LDS_AS bf16x8*)(e + 2048) = pack8<0>(acc[1], sc);
    *(LDS_AS bf16x8*)(e + 3072) = pack8<8>(acc[1], sc);
    if (w >= 4) {
      lds_ptr e2 = SW + L_EX + (32 + (w - 4) * 2) * 1024 + l * 16;
      *(LDS_AS bf16x8*)(e2) = pack8<0>(acc[2], 1.f);
      *(LDS_AS bf16x8*)(e2 + 1024) = pack8<8>(acc[2], 1.f);
    }
  }
  LBAR();
  const int p = w & 3, c2 = p >> 1, tt = p & 1, s2 = w >> 2;
  u32x32 st;
  {
    lds_cptr e = S + L_EX;
    bf16x8 qf = lds_b128(e + (8 + p * 2 + s2) * 1024 + l * 16);
    bf16x8 kf = lds_b128(e + (16 + p * 2 + s2) * 1024 + l * 16);
    bf16x8 kh = lds_b128(e + (32 + c2 * 2 + s2) * 1024 + (32 * h + 8 * tt + (n & 7)) * 16);
    int vl = (32 * h + 16 * s2 + (n & 15)) * 16;
    u32x4 v0 = *(const LDS_AS u32x4*)(e + (24 + p * 2) * 1024 + vl);
    u32x4 v1 = *(const LDS_AS u32x4*)(e + (25 + p * 2) * 1024 + vl);
    u32x2 vh = *(const LDS_AS u32x2*)(e + (36 + c2 * 2) * 1024 + vl + 8 * tt);
    u32x4 qa = *(const LDS_AS u32x4*)(e + (p * 2 + s2) * 1024 + l * 16);
    u32x4 qb = *(const LDS_AS u32x4*)(e + (p * 2 + (s2 ^ 1)) * 1024 + l * 16);
#pragma unroll
    for (int i = 0; i < 4; ++i) { st[i] = qa[i]; st[4 + i] = qb[i]; }
    f32x16 mc, mh = {0};
#pragma unroll
    for (int r = 0; r < 16; ++r) mc[r] = ((unsigned)(n - 4 * h - (8 * (r >> 2) + (r & 3))) <= 7u) ? 0.f : -__builtin_inff();
#pragma unroll
    for (int r = 0; r < 4; ++r) mh[r] = (4 * h + r > n && tb > 0) ? 0.f : -__builtin_inff();
    f32x16 sc = MFMA32(kf, qf, mc);
    f32x16 sh = MFMA32(kh, qf, mh);
    float m = fmaxf(fmaxf(max16(sc), fmaxf(sh[0], sh[1])), fmaxf(sh[2], sh[3]));
    m = xhalf_max(m);
#pragma unroll
    for (int r = 0; r < 16; ++r) sc[r] = __builtin_amdgcn_exp2f(sc[r] - m);
#pragma unroll
    for (int r = 0; r < 4; ++r) sh[r] = __builtin_amdgcn_exp2f(sh[r] - m);
    if (n >= 16) { v0 = u32x4{0x3f803f80u, 0x3f803f80u, 0x3f803f80u, 0x3f803f80u}; v1 = v0; vh = u32x2{0x3f803f80u, 0x3f803f80u}; }
    f32x16 o = MFMA32K8(__builtin_bit_cast(s16x4, vh), pack4(sh[0], sh[1], sh[2], sh[3]), f32x16{0});
    o = MFMA32(__builtin_bit_cast(bf16x8, v0), pack8<0>(sc, 1.f), o);
    o = MFMA32(__builtin_bit_cast(bf16x8, v1), pack8<8>(sc, 1.f), o);
    gst16_sc1(WS_O + ((2 * tb + tt) * 64 + 4 * hg + 2 * c2 + s2) * 1024 + l * 16, ws, __builtin_bit_cast(u32x4, pack8<0>(o, 1.f / o[8])));
  }
  asm volatile("s_waitcnt vmcnt(1) lgkmcnt(0)\n\ts_barrier"
               : "+v"(kv[0]), "+v"(kv[1]), "+v"(kv[2]), "+v"(kv[3]), "+v"(kv[4]), "+v"(kv[5]), "+v"(kv[6]), "+v"(kv[7]),
                 "+v"(kv[8]), "+v"(kv[9]), "+v"(kv[10]), "+v"(kv[11]), "+v"(kv[12]), "+v"(kv[13]), "+v"(kv[14]), "+v"(kv[15]) :: "memory");
#pragma unroll
  for (int i = 0; i < 16; ++i) *(LDS_AS u32x4*)(SW + w * 16384 + i * 1024 + l * 16) = kv[i];
  LBAR();
  const int tbo = 4 * xcd + (jj >> 3), cg = jj & 7;
  const unsigned ovo = (l >> 3) * 1024 + (((l & 7) ^ (((l >> 4) & 1) << 2)) * 16);
  float gq, go;
  f32x32 oo;
  float m0, m1;
  {
    const int kb = s2 * 65536 + l * 16;
    bf16x8 q0 = __builtin_bit_cast(bf16x8, u32x4{st[0], st[1], st[2], st[3]}), q1 = __builtin_bit_cast(bf16x8, u32x4{st[4], st[5], st[6], st[7]});
    bf16x8 k0 = lds_b128(S + kb);
    m0 = xhalf_max(max16(MFMA32(k0, q0, f32x16{0})));
    m1 = xhalf_max(max16(MFMA32(k0, q1, f32x16{0})));
    const unsigned ones = ((l & 27) == 16) ? 0x3f803f80u : 0u;
    f32x32 cc, ss;
    for (int pass = 0;; ++pass) {
#pragma unroll
      for (int i = 12; i < 28; ++i) st[i] = ones;
      st[20] = 0xdddd; st[21] = sb + kb;
#pragma unroll
      for (int i = 0; i < 16; ++i) { cc[i] = -m0; cc[16 + i] = -m1; }
#pragma unroll
      for (int i = 0; i < 32; ++i) { oo[i] = 0.f; ss[i] = 0.f; }
      att_pre(st, cc, ss, oo);
      att_seg(st, cc, ss, oo, 8);
      att_post(st, cc, ss, oo);
      bool ok = oo[8] < 1.2676506e30f && oo[24] < 1.2676506e30f;
#pragma unroll
      for (int i = 0; i < 8; ++i) ok = ok && (fabsf(oo[i]) < 3.0e38f) && (fabsf(oo[16 + i]) < 3.0e38f);
      if (pass == 1 || __builtin_amdgcn_ballot_w64(!ok) == 0) break;
      for (int t = 1; t < 32; ++t) {
        bf16x8 kt = lds_b128(S + kb + t * 2048);
        m0 = fmaxf(m0, max16(MFMA32(kt, q0, f32x16{0})));
        m1 = fmaxf(m1, max16(MFMA32(kt, q1, f32x16{0})));
      }
      m0 = xhalf_max(m0); m1 = xhalf_max(m1);
    }
  }
  {
    unsigned a, c;
    asm volatile("global_load_dword %0, %2, %4 sc1\n\tglobal_load_dword %1, %3, %4 sc1"
                 : "=&v"(a), "=&v"(c) : "v"(WS_G + (T0 + 2 * n + tt) * 4), "v"(WS_G + (64 * tbo + 2 * n + (w & 1)) * 4), "s"(ws) : "memory");
    lds_ptr mg = SW + L_MG + w * 3072 + l * 16;
    *(LDS_AS f32x4*)(mg) = f32x4{oo[16], oo[17], oo[18], oo[19]};
    *(LDS_AS f32x4*)(mg + 1024) = f32x4{oo[20], oo[21], oo[22], oo[23]};
    *(LDS_AS f32x4*)(mg + 2048) = f32x4{oo[24], m1, 0.f, 0.f};
    asm volatile("s_waitcnt vmcnt(0) lgkmcnt(0)\n\ts_barrier" : "+v"(a), "+v"(c) :: "memory");
    gq = __uint_as_float(a); go = __uint_as_float(c);
    if (w == 7 && l == 0) {
      gst8_sc1(WS_FA + (tb * 8 + hg) * 8, ws, u32x2{tlo, thi});
      if (b == 0) gst8_sc1(WS_EPOCH, ws, u32x2{tlo, thi});
    }
    lds_cptr mr = S + L_MG + (w ^ 4) * 3072 + l * 16;
    f32x4 r0 = *(const LDS_AS f32x4*)(mr), r1 = *(const LDS_AS f32x4*)(mr + 1024), r2 = *(const LDS_AS f32x4*)(mr + 2048);
    float mm = fmaxf(m0, r2[1]);
    float fa = __builtin_amdgcn_exp2f(m0 - mm), fb = __builtin_amdgcn_exp2f(r2[1] - mm);
    float f = (1.f - gq) / (oo[8] * fa + r2[0] * fb);
    fa *= f; fb *= f;
    bf16x8 ob;
#pragma unroll
    for (int j = 0; j < 4; ++j) { ob[j] = (__bf16)(oo[j] * fa + r0[j] * fb); ob[4 + j] = (__bf16)(oo[4 + j] * fa + r1[j] * fb); }
    gst16_sc1(WS_O + ((2 * tb + tt) * 64 + 32 + 4 * hg + 2 * c2 + s2) * 1024 + l * 16, ws, __builtin_bit_cast(u32x4, ob));
  }
  WAIT_BAR(0);
  if (w == 7 && l == 0) gst8_sc1(WS_FB + (tb * 8 + hg) * 8, ws, u32x2{tlo, thi});
  const int to = w & 1, kq = w >> 1;
  poll_flags(WS_FA + (tbo * 8 + 2 * kq + (l & 1)) * 8, ws, tlo, thi);
  u32x4 of[8];
#pragma unroll
  for (int i = 0; i < 8; ++i) of[i] = gld16_sc1(l * 16 + (i & 3) * 1024, ws + WS_O + ((2 * tbo + to) * 64 + 8 * kq + (i & 4)) * 1024);
#pragma unroll
  for (int i = 0; i < 8; ++i) dma16(ovo, (const char*)woa + 128 * cg + (64 * w + 8 * i) * 1024, sb + w * 8192 + i * 1024);
#pragma unroll
  for (int i = 0; i < 8; ++i) dma16(ovo, (const char*)wob + 128 * cg + (64 * w + 8 * i) * 1024, sb + 65536 + w * 8192 + i * 1024);
  asm volatile("s_waitcnt vmcnt(8)\n\ts_barrier"
               : "+v"(of[0]), "+v"(of[1]), "+v"(of[2]), "+v"(of[3]), "+v"(of[4]), "+v"(of[5]), "+v"(of[6]), "+v"(of[7]) :: "memory");
  int wo0 = kq * 16384 + (4 * h + q4) * 128 + (((2 * (g & 1) + (p4 >> 1)) ^ (((q4 >> 1) & 1) << 2)) * 16) + (p4 & 1) * 8;
  f32x16 a0 = {0}, a1 = {0};
#pragma unroll
  for (int i = 0; i < 8; ++i) {
    a0 = MFMA32(tr_frag(S + wo0 + i * 2048, 1024), __builtin_bit_cast(bf16x8, of[i]), a0);
    a1 = MFMA32(tr_frag(S + (wo0 ^ 64) + i * 2048, 1024), __builtin_bit_cast(bf16x8, of[i]), a1);
  }
  a0 *= go; a1 *= go;
  WAIT_BAR(0);
  poll_flags(WS_FB + (tbo * 8 + 2 * kq + (l & 1)) * 8, ws, tlo, thi);
#pragma unroll
  for (int i = 0; i < 8; ++i) of[i] = gld16_sc1(l * 16 + (i & 3) * 1024, ws + WS_O + ((2 * tbo + to) * 64 + 32 + 8 * kq + (i & 4)) * 1024);
  asm volatile("s_waitcnt vmcnt(0)"
               : "+v"(of[0]), "+v"(of[1]), "+v"(of[2]), "+v"(of[3]), "+v"(of[4]), "+v"(of[5]), "+v"(of[6]), "+v"(of[7]) :: "memory");
#pragma unroll
  for (int i = 0; i < 8; ++i) {
    a0 = MFMA32(tr_frag(S + 65536 + wo0 + i * 2048, 1024), __builtin_bit_cast(bf16x8, of[i]), a0);
    a1 = MFMA32(tr_frag(S + 65536 + (wo0 ^ 64) + i * 2048, 1024), __builtin_bit_cast(bf16x8, of[i]), a1);
  }
  {
    lds_ptr pw = SW + w * 8192 + n * 256;
#pragma unroll
    for (int i = 0; i < 4; ++i) {
      *(LDS_AS f32x4*)(pw + (((2 * i + h) ^ (n & 15)) * 16)) = f32x4{a0[4 * i], a0[4 * i + 1], a0[4 * i + 2], a0[4 * i + 3]};
      *(LDS_AS f32x4*)(pw + (((8 + 2 * i + h) ^ (n & 15)) * 16)) = f32x4{a1[4 * i], a1[4 * i + 1], a1[4 * i + 2], a1[4 * i + 3]};
    }
    LBAR();
    int nn = 8 * kq + (l >> 3);
    lds_cptr pr = S + to * 8192 + nn * 256;
    int u0 = ((2 * (l & 7)) ^ (nn & 15)) * 16;
    f32x4 s0 = {0, 0, 0, 0}, s1 = {0, 0, 0, 0};
#pragma unroll
    for (int k = 0; k < 4; ++k) { s0 += *(const LDS_AS f32x4*)(pr + k * 16384 + u0); s1 += *(const LDS_AS f32x4*)(pr + k * 16384 + (u0 ^ 16)); }
    bf16x8 r;
#pragma unroll
    for (int j = 0; j < 4; ++j) { r[j] = (__bf16)s0[j]; r[4 + j] = (__bf16)s1[j]; }
    *(bf16x8*)(out + (size_t)(64 * tbo + 2 * nn + to) * 512 + 64 * cg + 8 * (l & 7)) = r;
  }
}

extern "C" void kernel_launch(void* const* d_in, const int* in_sizes, int n_in, void* d_out, int out_size, void* d_ws,
                              size_t ws_size, hipStream_t stream) {
  k_fused<<<256, 512, 0, stream>>>((const u16*)d_in[0], (const u16*)d_in[1], (const u16*)d_in[2], (const u16*)d_in[4],
                                   (const u16*)d_in[5], (const u16*)d_in[6], (const u16*)d_in[7], (const u16*)d_in[8],
                                   (char*)d_ws, (u16*)d_out);
}
```

```cpp
#define PADK 0
#include <hip/hip_runtime.h>
#include <hip/hip_bf16.h>

typedef unsigned short u16;
typedef unsigned long long u64;
typedef __attribute__((ext_vector_type(4))) float f32x4;
typedef __attribute__((ext_vector_type(16))) float f32x16;
typedef __attribute__((ext_vector_type(32))) float f32x32;
typedef __attribute__((ext_vector_type(8))) __bf16 bf16x8;
typedef __attribute__((ext_vector_type(4))) __bf16 bf16x4;
typedef __attribute__((ext_vector_type(4))) short s16x4;
typedef __attribute__((ext_vector_type(8))) short s16x8;
typedef __attribute__((ext_vector_type(4))) unsigned u32x4;
typedef __attribute__((ext_vector_type(2))) unsigned u32x2;
typedef __attribute__((ext_vector_type(32))) unsigned u32x32;
#define LDS_AS __attribute__((address_space(3)))
typedef LDS_AS const char* lds_cptr;
typedef LDS_AS char* lds_ptr;

#define MFMA32(a, b, c) __builtin_amdgcn_mfma_f32_32x32x16_bf16(a, b, c, 0, 0, 0)
#define MFMA32K8(a, b, c) __builtin_amdgcn_mfma_f32_32x32x8bf16_1k(a, b, c, 0, 0, 0)
#define MFMA16(a, b, c) __builtin_amdgcn_mfma_f32_16x16x32_bf16(a, b, c, 0, 0, 0)
#define WAIT_BAR(N) asm volatile("s_waitcnt vmcnt(" #N ") lgkmcnt(0)\n\ts_barrier" ::: "memory")
#define LBAR() asm volatile("s_waitcnt lgkmcnt(0)\n\ts_barrier" ::: "memory")
#define PIN() __builtin_amdgcn_sched_barrier(0)

constexpr unsigned WS_EPOCH = 0;
constexpr unsigned WS_FKV = 4096;
constexpr unsigned WS_FA = 8192;
constexpr unsigned WS_FB = 12288;
constexpr unsigned WS_G = 16384;
constexpr unsigned WS_KV = 65536;
constexpr unsigned WS_O = 1u << 20;

#define STAMP_DECL unsigned long long pad_t0 = __builtin_amdgcn_s_memrealtime(), pad_tk = pad_t0
#define STAMP(idx) do { if ((idx) == PADK) pad_tk = __builtin_amdgcn_s_memrealtime(); } while (0)
#define STAMP_FLUSH(kid) do { unsigned long long tgt_ = pad_tk + 6000; for (int i_ = 0; i_ < 100000; ++i_) { if (__builtin_amdgcn_s_memrealtime() >= tgt_) break; __builtin_amdgcn_s_sleep(1); } } while (0)

constexpr float QSCALE = 0.25f * 1.4426950408889634f;

__device__ __forceinline__ void dma16(unsigned voff, const void* sbase, unsigned lds) {
  unsigned sv;
  asm volatile("s_mov_b32 %0, m0\n\ts_mov_b32 m0, %3\n\ts_nop 0\n\tglobal_load_lds_dwordx4 %1, %2\n\ts_mov_b32 m0, %0"
               : "=&s"(sv) : "v"(voff), "s"(sbase), "s"(lds) : "memory");
}
__device__ __forceinline__ u32x4 gld16(unsigned voff, const void* sbase) {
  u32x4 r;
  asm volatile("global_load_dwordx4 %0, %1, %2" : "=v"(r) : "v"(voff), "s"(sbase) : "memory");
  return r;
}
__device__ __forceinline__ u32x4 gld16_sc1(unsigned voff, const void* sbase) {
  u32x4 r;
  asm volatile("global_load_dwordx4 %0, %1, %2 sc1" : "=v"(r) : "v"(voff), "s"(sbase) : "memory");
  return r;
}
__device__ __forceinline__ void gst16_sc1(unsigned voff, void* sbase, u32x4 v) {
  asm volatile("global_store_dwordx4 %0, %1, %2 sc1\n\ts_nop 1" :: "v"(voff), "v"(v), "s"(sbase) : "memory");
}
__device__ __forceinline__ void gst8_sc1(unsigned voff, void* sbase, u32x2 v) {
  asm volatile("global_store_dwordx2 %0, %1, %2 sc1\n\ts_nop 1" :: "v"(voff), "v"(v), "s"(sbase) : "memory");
}
__device__ __forceinline__ void gst4_sc1(unsigned voff, void* sbase, unsigned v) {
  asm volatile("global_store_dword %0, %1, %2 sc1\n\ts_nop 1" :: "v"(voff), "v"(v), "s"(sbase) : "memory");
}
__device__ __forceinline__ void poll_flags(unsigned voff, const void* sbase, unsigned tlo, unsigned thi) {
  for (int it = 0; it < (1 << 20); ++it) {
    u32x2 f;
    asm volatile("global_load_dwordx2 %0, %1, %2 sc1\n\ts_waitcnt vmcnt(0)" : "=&v"(f) : "v"(voff), "s"(sbase) : "memory");
    if (__builtin_amdgcn_ballot_w64(f[0] != tlo || f[1] != thi) == 0) break;
    __builtin_amdgcn_s_sleep(2);
  }
}
__device__ __forceinline__ bf16x8 lds_b128(lds_cptr p) { return *(const LDS_AS bf16x8*)p; }
__device__ __forceinline__ s16x4 lds_tr(lds_cptr p) {
  return __builtin_amdgcn_ds_read_tr16_b64_v4i16((LDS_AS s16x4*)p);
}
__device__ __forceinline__ bf16x8 tr_frag(lds_cptr p, int off2) {
  s16x4 a = lds_tr(p), b = lds_tr(p + off2);
  s16x8 r = __builtin_shufflevector(a, b, 0, 1, 2, 3, 4, 5, 6, 7);
  return __builtin_bit_cast(bf16x8, r);
}
__device__ __forceinline__ s16x4 pack4(float a, float b, float c, float d) {
  bf16x4 r; r[0] = (__bf16)a; r[1] = (__bf16)b; r[2] = (__bf16)c; r[3] = (__bf16)d;
  return __builtin_bit_cast(s16x4, r);
}
template <int O> __device__ __forceinline__ bf16x8 pack8(const f32x16& a, float s) {
  bf16x8 r;
#pragma unroll
  for (int j = 0; j < 8; ++j) r[j] = (__bf16)(a[O + j] * s);
  return r;
}
__device__ __forceinline__ float xhalf_max(float v) {
  auto r = __builtin_amdgcn_permlane32_swap(__float_as_uint(v), __float_as_uint(v), false, false);
  return fmaxf(__uint_as_float(r[0]), __uint_as_float(r[1]));
}
__device__ __forceinline__ float max16(const f32x16& s) {
  float a = fmaxf(fmaxf(s[0], s[1]), s[2]);
#pragma unroll
  for (int i = 3; i < 15; i += 2) a = fmaxf(fmaxf(a, s[i]), s[i + 1]);
  return fmaxf(a, s[15]);
}

#define STR2(x) #x
#define STR(x) STR2(x)
#define EX(r) "v_exp_f32 v" STR(r) ", v" STR(r) "\n\t"
#define SM(O, V, S) "v_smfmac_f32_32x32x32_bf16 " O ", " V ", " S ", v84\n\t"
#define MF(S, Q, C) "v_mfma_f32_32x32x16_bf16 " S ", v[72:75], " Q ", " C "\n\t"
#define T64(V1P, V1C, KOFF, VOFF0, VOFF1) \
  EX(128) EX(129) EX(130) \
  SM("v[176:191]", V1P, "v[152:159]") \
  EX(131) EX(132) EX(133) EX(134) EX(135) \
  MF("v[144:159]", "v[68:71]", "v[112:127]") \
  "ds_read_b128 v[72:75], v85 offset:" STR(KOFF) "\n\t" \
  EX(136) EX(137) EX(138) EX(139) EX(140) \
  "s_waitcnt lgkmcnt(1)\n\t" \
  SM("v[160:175]", "v[76:79]", "v[128:135]") \
  EX(141) EX(142) EX(143) \
  EX(144) EX(145) EX(146) \
  SM("v[160:175]", V1C, "v[136:143]") \
  EX(147) EX(148) EX(149) EX(150) EX(151) \
  "s_waitcnt lgkmcnt(0)\n\t" \
  MF("v[128:143]", "v[64:67]", "v[96:111]") \
  EX(152) EX(153) EX(154) EX(155) EX(156) \
  SM("v[176:191]", "v[76:79]", "v[144:151]") \
  "s_mov_b64 exec, %[mask]\n\t" \
  "ds_read_b128 v[76:79], v85 offset:" STR(VOFF0) "\n\t" \
  "ds_read_b128 " V1P ", v85 offset:" STR(VOFF1) "\n\t" \
  "s_mov_b64 exec, -1\n\t" \
  EX(157) EX(158) EX(159)
#define ATT_OPS "+{v[64:95]}"(st), "+{v[96:127]}"(cc), "+{v[128:159]}"(ss), "+{v[160:191]}"(oo)
constexpr u64 VMASK = 0x0000FFFF0000FFFFull;

__device__ __forceinline__ void att_pre(u32x32& st, f32x32& cc, f32x32& ss, f32x32& oo) {
  asm volatile(
      "ds_read_b128 v[72:75], v85\n\t"
      "s_mov_b64 exec, %[mask]\n\t"
      "ds_read_b128 v[76:79], v85 offset:1024\n\t"
      "ds_read_b128 v[80:83], v85 offset:1280\n\t"
      "s_mov_b64 exec, -1\n\t"
      "s_waitcnt lgkmcnt(0)\n\t"
      MF("v[128:143]", "v[64:67]", "v[96:111]")
      "s_nop 15\n\t"
      : ATT_OPS : [mask] "s"(VMASK) : "memory");
}
__device__ __forceinline__ void att_seg(u32x32& st, f32x32& cc, f32x32& ss, f32x32& oo, unsigned n4) {
  asm volatile(
      "LOOP%=:\n\t"
      T64("v[88:91]", "v[80:83]", 2048, 3072, 3328)
      T64("v[80:83]", "v[88:91]", 4096, 5120, 5376)
      T64("v[88:91]", "v[80:83]", 6144, 7168, 7424)
      T64("v[80:83]", "v[88:91]", 8192, 9216, 9472)
      "v_add_u32 v85, 0x2000, v85\n\t"
      "s_sub_u32 %[cnt], %[cnt], 1\n\t"
      "s_cmp_lg_u32 %[cnt], 0\n\t"
      "s_cbranch_scc1 LOOP%=\n\t"
      "s_waitcnt lgkmcnt(0)\n\t"
      : ATT_OPS, [cnt] "+s"(n4) : [mask] "s"(VMASK) : "memory", "scc");
}
__device__ __forceinline__ void att_post(u32x32& st, f32x32& cc, f32x32& ss, f32x32& oo) {
  asm volatile(
      SM("v[176:191]", "v[88:91]", "v[152:159]")
      "s_nop 15\n\ts_nop 7\n\t"
      : ATT_OPS :: "memory");
}

constexpr int L_XM = 0, L_XH = 65536, L_WR = 81920, L_WKV = 49152;
constexpr int L_EX = 122880;
constexpr int L_MG = 131072;

template <int ROLE>
__device__ __forceinline__ void proj_step(lds_cptr S, int wbase, int xbase, int hbase, int par, f32x16 (&acc)[3]) {
#pragma unroll
  for (int ks = 0; ks < 2; ++ks) {
    bf16x8 wf = tr_frag(S + wbase + ks * 2048, 512);
    int xo = (2 * par + ks) << 5;
    bf16x8 x0 = lds_b128(S + (xbase ^ xo)), x1 = lds_b128(S + (xbase ^ xo) + 4096);
    if (ROLE == 2) { acc[0] = MFMA32(x0, wf, acc[0]); acc[1] = MFMA32(x1, wf, acc[1]); }
    else { acc[0] = MFMA32(wf, x0, acc[0]); acc[1] = MFMA32(wf, x1, acc[1]); }
    if (ROLE != 0) {
      bf16x8 xh = lds_b128(S + (hbase ^ xo));
      if (ROLE == 2) acc[2] = MFMA32(xh, wf, acc[2]); else acc[2] = MFMA32(wf, xh, acc[2]);
    }
  }
}

__global__ __launch_bounds__(512, 2) void k_fused(const u16* __restrict__ x, const u16* __restrict__ wqkv,
                                                  const u16* __restrict__ woa, const u16* __restrict__ wq,
                                                  const u16* __restrict__ wk, const u16* __restrict__ wv,
                                                  const u16* __restrict__ wob, const u16* __restrict__ gw,
                                                  char* ws, u16* __restrict__ out) {
  __shared__ __attribute__((aligned(1024))) char smem[163840];
  const unsigned sb = (unsigned)(size_t)(LDS_AS char*)smem;
  lds_cptr S = (lds_cptr)smem;
  lds_ptr SW = (lds_ptr)smem;
  const int b = blockIdx.x, xcd = b & 7, jj = b >> 3;
  const int tb = 8 * (xcd >> 1) + (jj >> 2), hg = 4 * (xcd & 1) + (jj & 3);
  const int rot = (tb + 2 * (jj & 3)) & 7;
  const int T0 = 64 * tb;
  const int l = threadIdx.x & 63;
  const int w = __builtin_amdgcn_readfirstlane(threadIdx.x >> 6);
  const int g = l >> 4, q4 = (l & 15) >> 2, p4 = l & 3, h = l >> 5, n = l & 31;
  STAMP_DECL; STAMP(0);

  u32x2 ep;
  asm volatile("global_load_dwordx2 %0, %1, %2 sc1" : "=v"(ep) : "v"(0u), "s"(ws + WS_EPOCH) : "memory");
  const unsigned kxo = (unsigned)((T0 + 16 * (hg >> 1) + (l & 15)) * 1024 + 128 * w + 16 * g);
  u32x4 kx0 = gld16(kxo, x), kx1 = gld16(kxo + 64, x);
  u32x4 gw0 = gld16(128 * w + 16 * g, gw), gw1 = gld16(128 * w + 64 + 16 * g, gw);
  {
    const char* wkv = (const char*)((hg & 1) ? wv : wk) + 2048 * w;
    dma16(l * 16, wkv, sb + L_WKV + 2048 * w);
    dma16(l * 16, wkv + 1024, sb + L_WKV + 2048 * w + 1024);
  }
  const int seg = w >> 1, half = w & 1;
  const int ld2 = (w == 0) ? 1024 : 3072;
  const char* wsrc = (w == 0 ? (const char*)wq : (const char*)wqkv + (w - 1) * 1024) + 128 * hg;
  const unsigned wvo = (l >> 3) * ld2 + (((l & 7) ^ (((l >> 4) & 1) << 2)) * 16);
  unsigned xvo[2];
#pragma unroll
  for (int i = 0; i < 2; ++i) {
    int r = 8 * (2 * (w & 3) + i) + (l >> 3);
    int t = T0 + 2 * (r & 31) + (r >> 5);
    xvo[i] = t * 1024 + (((l & 7) ^ ((r >> 1) & 7)) * 16);
  }
  auto stage_w = [&](int c, int slot) {
    int cc = (c + 2 * rot) & 15;
    const char* p = wsrc + (size_t)cc * 32 * ld2;
#pragma unroll
    for (int i = 0; i < 4; ++i) dma16(wvo, p + i * 8 * ld2, sb + L_WR + slot * 16384 + w * 4096 + i * 1024);
  };
  auto stage_x = [&](int j) {
    const char* p = (const char*)x + ((j + rot) & 7) * 128;
    dma16(xvo[0], p, sb + L_XM + j * 8192 + w * 2048);
    dma16(xvo[1], p, sb + L_XM + j * 8192 + w * 2048 + 1024);
  };
  if (w < 4) {
    stage_x(0); stage_w(0, 0); stage_w(1, 1); stage_x(1); stage_w(2, 2); stage_w(3, 3); stage_x(2); stage_w(4, 4);
    asm volatile("s_waitcnt vmcnt(26)" ::: "memory");
  } else {
#pragma unroll
    for (int i = 0; i < 2; ++i) {
      int r = 8 * i + (l >> 3);
      int t = T0 - 16 + 2 * (r & 7) + (r >> 3);
      t = t < 0 ? 0 : t;
      unsigned vo = t * 1024 + (((l & 7) ^ ((r >> 1) & 7)) * 16);
#pragma unroll
      for (int jx = 0; jx < 2; ++jx) {
        int j = 2 * (w - 4) + jx;
        dma16(vo, (const char*)x + ((j + rot) & 7) * 128, sb + L_XH + j * 2048 + i * 1024);
      }
    }
    asm volatile("s_waitcnt vmcnt(4)" ::: "memory");
  }
  asm volatile("" : "+v"(ep), "+v"(kx0), "+v"(kx1), "+v"(gw0), "+v"(gw1) :: "memory");
  STAMP(1);
  const u64 tag = (((u64)__builtin_amdgcn_readfirstlane(ep[1]) << 32) | __builtin_amdgcn_readfirstlane(ep[0])) * 6364136223846793005ull + 1442695040888963407ull;
  const unsigned tlo = (unsigned)tag, thi = (unsigned)(tag >> 32);
  {
    lds_cptr wp = S + L_WKV + 2048 * w + (8 * g + q4) * 32 + p4 * 8;
    bf16x8 w0 = tr_frag(wp, 128), w1 = tr_frag(wp + 1024, 128);
    bf16x8 a0 = __builtin_bit_cast(bf16x8, kx0), a1 = __builtin_bit_cast(bf16x8, kx1);
    f32x4 z = {0, 0, 0, 0}, d;
    if (hg & 1) d = MFMA16(a1, w1, MFMA16(a0, w0, z)); else d = MFMA16(w1, a1, MFMA16(w0, a0, z));
    f32x4 dg = MFMA16(__builtin_bit_cast(bf16x8, gw1), a1, MFMA16(__builtin_bit_cast(bf16x8, gw0), a0, z));
    *(LDS_AS f32x4*)(SW + L_WKV + 2048 * w + l * 16) = d;
    *(LDS_AS float*)(SW + L_WKV + 2048 * w + 1024 + l * 4) = dg[0];
  }
  f32x16 acc[3];
  acc[0] = f32x16{0}; acc[1] = f32x16{0}; acc[2] = f32x16{0};
  const int woff = L_WR + seg * 4096 + (8 * h + q4) * 128 + (((4 * half + 2 * (g & 1) + (p4 >> 1)) ^ (((q4 >> 1) & 1) << 2)) * 16) + (p4 & 1) * 8;
  const int xoff = L_XM + n * 128 + ((h ^ ((n >> 1) & 7)) * 16);
  const int hoff = L_XH + (n & 15) * 128 + ((h ^ ((n >> 1) & 7)) * 16);
  if (w < 4) {
    int slot = 0;
    for (int j = 0; j < 8; ++j) {
      if (j == 0) WAIT_BAR(20); else if (j < 7) WAIT_BAR(14); else WAIT_BAR(4);
      int prev = slot == 0 ? 4 : slot - 1;
      if (j > 0 && j < 6) { stage_x(j + 2); stage_w(2 * j + 4, prev); }
      proj_step<0>(S, woff + slot * 16384, xoff + j * 8192, 0, 0, acc);
      prev = slot; slot = slot == 4 ? 0 : slot + 1;
      if (j < 6) WAIT_BAR(16); else if (j == 6) WAIT_BAR(10); else WAIT_BAR(0);
      if (j < 6) stage_w(2 * j + 5, prev);
      proj_step<0>(S, woff + slot * 16384, xoff + j * 8192, 0, 1, acc);
      slot = slot == 4 ? 0 : slot + 1;
    }
  } else {
    WAIT_BAR(0);
    if (w == 7) {
      f32x4 d = *(const LDS_AS f32x4*)(S + L_WKV + l * 16);
      float dg = *(const LDS_AS float*)(S + L_WKV + 1024 + l * 4);
#pragma unroll
      for (int i = 1; i < 8; ++i) { d += *(const LDS_AS f32x4*)(S + L_WKV + 2048 * i + l * 16); dg += *(const LDS_AS float*)(S + L_WKV + 2048 * i + 1024 + l * 4); }
      u32x2 pk = __builtin_bit_cast(u32x2, pack4(d[0], d[1], d[2], d[3]));
      int tk = T0 + 16 * (hg >> 1), sblk = (hg >> 1) & 1;
      unsigned o = WS_KV + (tk >> 5) * 2048 + ((hg & 1) ? 1024 + (g >> 1) * 512 + sblk * 256 + (l & 15) * 16 + (g & 1) * 8
                                                        : (g & 1) * 512 + (16 * sblk + (l & 15)) * 16 + (g >> 1) * 8);
      gst8_sc1(o, ws, pk);
      if ((hg & 1) && l < 16) gst4_sc1(WS_G + (tk + l) * 4, ws, __float_as_uint(1.f / (1.f + __expf(-dg))));
    }
    int slot = 0;
    for (int j = 0; j < 8; ++j) {
      if (j > 0) LBAR();
      if (w < 6) proj_step<1>(S, woff + slot * 16384, xoff + j * 8192, hoff + j * 2048, 0, acc);
      else proj_step<2>(S, woff + slot * 16384, xoff + j * 8192, hoff + j * 2048, 0, acc);
      slot = slot == 4 ? 0 : slot + 1;
      if (j == 1 && w == 7) {
        asm volatile("s_waitcnt vmcnt(0)" ::: "memory");
        if (l == 0) gst8_sc1(WS_FKV + (tb * 8 + hg) * 8, ws, u32x2{tlo, thi});
        STAMP(2);
      }
      LBAR();
      if (w < 6) proj_step<1>(S, woff + slot * 16384, xoff + j * 8192, hoff + j * 2048, 1, acc);
      else proj_step<2>(S, woff + slot * 16384, xoff + j * 8192, hoff + j * 2048, 1, acc);
      slot = slot == 4 ? 0 : slot + 1;
    }
  }
  STAMP(3);
  poll_flags(WS_FKV + (w * 32 + n) * 8, ws, tlo, thi);
  STAMP(4);
  u32x4 kv[16];
#pragma unroll
  for (int i = 0; i < 16; ++i) kv[i] = gld16_sc1(l * 16 + (i & 3) * 1024, ws + WS_KV + w * 16384 + (i >> 2) * 4096);
  LBAR();
  {
    float sc = w < 4 ? QSCALE : 1.f;
    lds_ptr e = SW + L_EX + (seg * 8 + half * 4) * 1024 + l * 16;
    *(LDS_AS bf16x8*)(e) = pack8<0>(acc[0], sc);
    *(LDS_AS bf16x8*)(e + 1024) = pack8<8>(acc[0], sc);
    *(LDS_AS bf16x8*)(e + 2048) = pack8<0>(acc[1], sc);
    *(LDS_AS bf16x8*)(e + 3072) = pack8<8>(acc[1], sc);
    if (w >= 4) {
      lds_ptr e2 = SW + L_EX + (32 + (w - 4) * 2) * 1024 + l * 16;
      *(LDS_AS bf16x8*)(e2) = pack8<0>(acc[2], 1.f);
      *(LDS_AS bf16x8*)(e2 + 1024) = pack8<8>(acc[2], 1.f);
    }
  }
  LBAR();
  STAMP(5);
  const int p = w & 3, c2 = p >> 1, tt = p & 1, s2 = w >> 2;
  u32x32 st;
  {
    lds_cptr e = S + L_EX;
    bf16x8 qf = lds_b128(e + (8 + p * 2 + s2) * 1024 + l * 16);
    bf16x8 kf = lds_b128(e + (16 + p * 2 + s2) * 1024 + l * 16);
    bf16x8 kh = lds_b128(e + (32 + c2 * 2 + s2) * 1024 + (32 * h + 8 * tt + (n & 7)) * 16);
    int vl = (32 * h + 16 * s2 + (n & 15)) * 16;
    u32x4 v0 = *(const LDS_AS u32x4*)(e + (24 + p * 2) * 1024 + vl);
    u32x4 v1 = *(const LDS_AS u32x4*)(e + (25 + p * 2) * 1024 + vl);
    u32x2 vh = *(const LDS_AS u32x2*)(e + (36 + c2 * 2) * 1024 + vl + 8 * tt);
    u32x4 qa = *(const LDS_AS u32x4*)(e + (p * 2 + s2) * 1024 + l * 16);
    u32x4 qb = *(const LDS_AS u32x4*)(e + (p * 2 + (s2 ^ 1)) * 1024 + l * 16);
#pragma unroll
    for (int i = 0; i < 4; ++i) { st[i] = qa[i]; st[4 + i] = qb[i]; }
    f32x16 mc, mh = {0};
#pragma unroll
    for (int r = 0; r < 16; ++r) mc[r] = ((unsigned)(n - 4 * h - (8 * (r >> 2) + (r & 3))) <= 7u) ? 0.f : -__builtin_inff();
#pragma unroll
    for (int r = 0; r < 4; ++r) mh[r] = (4 * h + r > n && tb > 0) ? 0.f : -__builtin_inff();
    f32x16 sc = MFMA32(kf, qf, mc);
    f32x16 sh = MFMA32(kh, qf, mh);
    float m = fmaxf(fmaxf(max16(sc), fmaxf(sh[0], sh[1])), fmaxf(sh[2], sh[3]));
    m = xhalf_max(m);
#pragma unroll
    for (int r = 0; r < 16; ++r) sc[r] = __builtin_amdgcn_exp2f(sc[r] - m);
#pragma unroll
    for (int r = 0; r < 4; ++r) sh[r] = __builtin_amdgcn_exp2f(sh[r] - m);
    if (n >= 16) { v0 = u32x4{0x3f803f80u, 0x3f803f80u, 0x3f803f80u, 0x3f803f80u}; v1 = v0; vh = u32x2{0x3f803f80u, 0x3f803f80u}; }
    f32x16 o = MFMA32K8(__builtin_bit_cast(s16x4, vh), pack4(sh[0], sh[1], sh[2], sh[3]), f32x16{0});
    o = MFMA32(__builtin_bit_cast(bf16x8, v0), pack8<0>(sc, 1.f), o);
    o = MFMA32(__builtin_bit_cast(bf16x8, v1), pack8<8>(sc, 1.f), o);
    gst16_sc1(WS_O + ((2 * tb + tt) * 64 + 4 * hg + 2 * c2 + s2) * 1024 + l * 16, ws, __builtin_bit_cast(u32x4, pack8<0>(o, 1.f / o[8])));
  }
  STAMP(6);
  asm volatile("s_waitcnt vmcnt(1) lgkmcnt(0)\n\ts_barrier"
               : "+v"(kv[0]), "+v"(kv[1]), "+v"(kv[2]), "+v"(kv[3]), "+v"(kv[4]), "+v"(kv[5]), "+v"(kv[6]), "+v"(kv[7]),
                 "+v"(kv[8]), "+v"(kv[9]), "+v"(kv[10]), "+v"(kv[11]), "+v"(kv[12]), "+v"(kv[13]), "+v"(kv[14]), "+v"(kv[15]) :: "memory");
#pragma unroll
  for (int i = 0; i < 16; ++i) *(LDS_AS u32x4*)(SW + w * 16384 + i * 1024 + l * 16) = kv[i];
  LBAR();
  STAMP(7);
  const int tbo = 4 * xcd + (jj >> 3), cg = jj & 7;
  const unsigned ovo = (l >> 3) * 1024 + (((l & 7) ^ (((l >> 4) & 1) << 2)) * 16);
  float gq, go;
  f32x32 oo;
  float m0, m1;
  {
    const int kb = s2 * 65536 + l * 16;
    bf16x8 q0 = __builtin_bit_cast(bf16x8, u32x4{st[0], st[1], st[2], st[3]}), q1 = __builtin_bit_cast(bf16x8, u32x4{st[4], st[5], st[6], st[7]});
    bf16x8 k0 = lds_b128(S + kb);
    m0 = xhalf_max(max16(MFMA32(k0, q0, f32x16{0})));
    m1 = xhalf_max(max16(MFMA32(k0, q1, f32x16{0})));
    const unsigned ones = ((l & 27) == 16) ? 0x3f803f80u : 0u;
    f32x32 cc, ss;
    for (int pass = 0;; ++pass) {
#pragma unroll
      for (int i = 12; i < 28; ++i) st[i] = ones;
      st[20] = 0xdddd; st[21] = sb + kb;
#pragma unroll
      for (int i = 0; i < 16; ++i) { cc[i] = -m0; cc[16 + i] = -m1; }
#pragma unroll
      for (int i = 0; i < 32; ++i) { oo[i] = 0.f; ss[i] = 0.f; }
      att_pre(st, cc, ss, oo);
      att_seg(st, cc, ss, oo, 8);
      att_post(st, cc, ss, oo);
      bool ok = oo[8] < 1.2676506e30f && oo[24] < 1.2676506e30f;
#pragma unroll
      for (int i = 0; i < 8; ++i) ok = ok && (fabsf(oo[i]) < 3.0e38f) && (fabsf(oo[16 + i]) < 3.0e38f);
      if (pass == 1 || __builtin_amdgcn_ballot_w64(!ok) == 0) break;
      for (int t = 1; t < 32; ++t) {
        bf16x8 kt = lds_b128(S + kb + t * 2048);
        m0 = fmaxf(m0, max16(MFMA32(kt, q0, f32x16{0})));
        m1 = fmaxf(m1, max16(MFMA32(kt, q1, f32x16{0})));
      }
      m0 = xhalf_max(m0); m1 = xhalf_max(m1);
    }
  }
  STAMP(8);
  {
    unsigned a, c;
    asm volatile("global_load_dword %0, %2, %4 sc1\n\tglobal_load_dword %1, %3, %4 sc1"
                 : "=&v"(a), "=&v"(c) : "v"(WS_G + (T0 + 2 * n + tt) * 4), "v"(WS_G + (64 * tbo + 2 * n + (w & 1)) * 4), "s"(ws) : "memory");
    lds_ptr mg = SW + L_MG + w * 3072 + l * 16;
    *(LDS_AS f32x4*)(mg) = f32x4{oo[16], oo[17], oo[18], oo[19]};
    *(LDS_AS f32x4*)(mg + 1024) = f32x4{oo[20], oo[21], oo[22], oo[23]};
    *(LDS_AS f32x4*)(mg + 2048) = f32x4{oo[24], m1, 0.f, 0.f};
    asm volatile("s_waitcnt vmcnt(0) lgkmcnt(0)\n\ts_barrier" : "+v"(a), "+v"(c) :: "memory");
    gq = __uint_as_float(a); go = __uint_as_float(c);
    if (w == 7 && l == 0) {
      gst8_sc1(WS_FA + (tb * 8 + hg) * 8, ws, u32x2{tlo, thi});
      if (b == 0) gst8_sc1(WS_EPOCH, ws, u32x2{tlo, thi});
    }
    lds_cptr mr = S + L_MG + (w ^ 4) * 3072 + l * 16;
    f32x4 r0 = *(const LDS_AS f32x4*)(mr), r1 = *(const LDS_AS f32x4*)(mr + 1024), r2 = *(const LDS_AS f32x4*)(mr + 2048);
    float mm = fmaxf(m0, r2[1]);
    float fa = __builtin_amdgcn_exp2f(m0 - mm), fb = __builtin_amdgcn_exp2f(r2[1] - mm);
    float f = (1.f - gq) / (oo[8] * fa + r2[0] * fb);
    fa *= f; fb *= f;
    bf16x8 ob;
#pragma unroll
    for (int j = 0; j < 4; ++j) { ob[j] = (__bf16)(oo[j] * fa + r0[j] * fb); ob[4 + j] = (__bf16)(oo[4 + j] * fa + r1[j] * fb); }
    gst16_sc1(WS_O + ((2 * tb + tt) * 64 + 32 + 4 * hg + 2 * c2 + s2) * 1024 + l * 16, ws, __builtin_bit_cast(u32x4, ob));
  }
  WAIT_BAR(0);
  if (w == 7 && l == 0) gst8_sc1(WS_FB + (tb * 8 + hg) * 8, ws, u32x2{tlo, thi});
  STAMP(9);
  const int to = w & 1, kq = w >> 1;
  poll_flags(WS_FA + (tbo * 8 + 2 * kq + (l & 1)) * 8, ws, tlo, thi);
  u32x4 of[8];
#pragma unroll
  for (int i = 0; i < 8; ++i) of[i] = gld16_sc1(l * 16 + (i & 3) * 1024, ws + WS_O + ((2 * tbo + to) * 64 + 8 * kq + (i & 4)) * 1024);
#pragma unroll
  for (int i = 0; i < 8; ++i) dma16(ovo, (const char*)woa + 128 * cg + (64 * w + 8 * i) * 1024, sb + w * 8192 + i * 1024);
#pragma unroll
  for (int i = 0; i < 8; ++i) dma16(ovo, (const char*)wob + 128 * cg + (64 * w + 8 * i) * 1024, sb + 65536 + w * 8192 + i * 1024);
  asm volatile("s_waitcnt vmcnt(8)\n\ts_barrier"
               : "+v"(of[0]), "+v"(of[1]), "+v"(of[2]), "+v"(of[3]), "+v"(of[4]), "+v"(of[5]), "+v"(of[6]), "+v"(of[7]) :: "memory");
  STAMP(10);
  int wo0 = kq * 16384 + (4 * h + q4) * 128 + (((2 * (g & 1) + (p4 >> 1)) ^ (((q4 >> 1) & 1) << 2)) * 16) + (p4 & 1) * 8;
  f32x16 a0 = {0}, a1 = {0};
#pragma unroll
  for (int i = 0; i < 8; ++i) {
    a0 = MFMA32(tr_frag(S + wo0 + i * 2048, 1024), __builtin_bit_cast(bf16x8, of[i]), a0);
    a1 = MFMA32(tr_frag(S + (wo0 ^ 64) + i * 2048, 1024), __builtin_bit_cast(bf16x8, of[i]), a1);
  }
  a0 *= go; a1 *= go;
  WAIT_BAR(0);
  STAMP(11);
  poll_flags(WS_FB + (tbo * 8 + 2 * kq + (l & 1)) * 8, ws, tlo, thi);
  STAMP(12);
#pragma unroll
  for (int i = 0; i < 8; ++i) of[i] = gld16_sc1(l * 16 + (i & 3) * 1024, ws + WS_O + ((2 * tbo + to) * 64 + 32 + 8 * kq + (i & 4)) * 1024);
  asm volatile("s_waitcnt vmcnt(0)"
               : "+v"(of[0]), "+v"(of[1]), "+v"(of[2]), "+v"(of[3]), "+v"(of[4]), "+v"(of[5]), "+v"(of[6]), "+v"(of[7]) :: "memory");
  STAMP(13);
#pragma unroll
  for (int i = 0; i < 8; ++i) {
    a0 = MFMA32(tr_frag(S + 65536 + wo0 + i * 2048, 1024), __builtin_bit_cast(bf16x8, of[i]), a0);
    a1 = MFMA32(tr_frag(S + 65536 + (wo0 ^ 64) + i * 2048, 1024), __builtin_bit_cast(bf16x8, of[i]), a1);
  }
  {
    lds_ptr pw = SW + w * 8192 + n * 256;
#pragma unroll
    for (int i = 0; i < 4; ++i) {
      *(LDS_AS f32x4*)(pw + (((2 * i + h) ^ (n & 15)) * 16)) = f32x4{a0[4 * i], a0[4 * i + 1], a0[4 * i + 2], a0[4 * i + 3]};
      *(LDS_AS f32x4*)(pw + (((8 + 2 * i + h) ^ (n & 15)) * 16)) = f32x4{a1[4 * i], a1[4 * i + 1], a1[4 * i + 2], a1[4 * i + 3]};
    }
    LBAR();
    STAMP(14);
    int nn = 8 * kq + (l >> 3);
    lds_cptr pr = S + to * 8192 + nn * 256;
    int u0 = ((2 * (l & 7)) ^ (nn & 15)) * 16;
    f32x4 s0 = {0, 0, 0, 0}, s1 = {0, 0, 0, 0};
#pragma unroll
    for (int k = 0; k < 4; ++k) { s0 += *(const LDS_AS f32x4*)(pr + k * 16384 + u0); s1 += *(const LDS_AS f32x4*)(pr + k * 16384 + (u0 ^ 16)); }
    bf16x8 r;
#pragma unroll
    for (int j = 0; j < 4; ++j) { r[j] = (__bf16)s0[j]; r[4 + j] = (__bf16)s1[j]; }
    asm volatile("global_store_dwordx4 %0, %1, %2 \n\ts_nop 1" :: "v"((unsigned)(((64 * tbo + 2 * nn + to) * 512 + 64 * cg + 8 * (l & 7)) * 2)), "v"(__builtin_bit_cast(u32x4, r)), "s"(out) : "memory");
    asm volatile("s_waitcnt vmcnt(0)\n\tbuffer_wbl2 sc0 sc1\n\ts_waitcnt vmcnt(0)" ::: "memory");
  }
  STAMP(15); STAMP_FLUSH(0);
}

extern "C" void kernel_launch(void* const* d_in, const int* in_sizes, int n_in, void* d_out, int out_size, void* d_ws,
                              size_t ws_size, hipStream_t stream) {
  k_fused<<<256, 512, 0, stream>>>((const u16*)d_in[0], (const u16*)d_in[1], (const u16*)d_in[2], (const u16*)d_in[4],
                                   (const u16*)d_in[5], (const u16*)d_in[6], (const u16*)d_in[7], (const u16*)d_in[8],
                                   (char*)d_ws, (u16*)d_out);
}
```

```cpp
#include <hip/hip_runtime.h>
#include <hip/hip_bf16.h>

typedef unsigned short u16;
typedef unsigned long long u64;
typedef __attribute__((ext_vector_type(2))) float f32x2;
typedef __attribute__((ext_vector_type(2))) __bf16 bf16x2;
typedef __attribute__((ext_vector_type(4))) float f32x4;
typedef __attribute__((ext_vector_type(8))) float f32x8;
typedef __attribute__((ext_vector_type(16))) float f32x16;
typedef __attribute__((ext_vector_type(32))) float f32x32;
typedef __attribute__((ext_vector_type(16))) __bf16 bf16x16;
typedef __attribute__((ext_vector_type(8))) __bf16 bf16x8;
typedef __attribute__((ext_vector_type(4))) __bf16 bf16x4;
typedef __attribute__((ext_vector_type(4))) short s16x4;
typedef __attribute__((ext_vector_type(8))) short s16x8;
typedef __attribute__((ext_vector_type(4))) unsigned u32x4;
typedef __attribute__((ext_vector_type(2))) unsigned u32x2;
typedef __attribute__((ext_vector_type(8))) unsigned u32x8;
typedef __attribute__((ext_vector_type(16))) unsigned u32x16;
#define LDS_AS __attribute__((address_space(3)))
typedef LDS_AS const char* lds_cptr;
typedef LDS_AS char* lds_ptr;

#define MFMA32(a, b, c) __builtin_amdgcn_mfma_f32_32x32x16_bf16(a, b, c, 0, 0, 0)
#define MFMA16(a, b, c) __builtin_amdgcn_mfma_f32_16x16x32_bf16(a, b, c, 0, 0, 0)
#define MFMA16K(a, b, c) __builtin_amdgcn_mfma_f32_16x16x16bf16_1k(a, b, c, 0, 0, 0)
#define WAIT_BAR(N) asm volatile("s_waitcnt vmcnt(" #N ") lgkmcnt(0)\n\ts_barrier" ::: "memory")
#define LBAR() asm volatile("s_waitcnt lgkmcnt(0)\n\ts_barrier" ::: "memory")
#define BAR() asm volatile("s_barrier" ::: "memory")
#define STR2(x) #x
#define STR(x) STR2(x)

constexpr unsigned WS_EPOCH = 0;
constexpr unsigned WS_FKV = 4096;
constexpr unsigned WS_G = 32768;
constexpr unsigned WS_KV = 65536;
constexpr unsigned WS_FA = 262144;
constexpr unsigned WS_FB = 327680;
constexpr unsigned WS_O = 1u << 20;

constexpr float QSCALE = 0.25f * 1.4426950408889634f;

__device__ __forceinline__ void dma4(unsigned v0, unsigned v1, unsigned v2, unsigned v3, const void* sbase, unsigned lds) {
  unsigned sv;
  asm volatile("s_mov_b32 %0, m0\n\ts_mov_b32 m0, %6\n\ts_nop 0\n\tglobal_load_lds_dwordx4 %1, %5\n\t"
               "s_add_u32 m0, m0, 0x400\n\ts_nop 0\n\tglobal_load_lds_dwordx4 %2, %5\n\t"
               "s_add_u32 m0, m0, 0x400\n\ts_nop 0\n\tglobal_load_lds_dwordx4 %3, %5\n\t"
               "s_add_u32 m0, m0, 0x400\n\ts_nop 0\n\tglobal_load_lds_dwordx4 %4, %5\n\ts_mov_b32 m0, %0"
               : "=&s"(sv) : "v"(v0), "v"(v1), "v"(v2), "v"(v3), "s"(sbase), "s"(lds) : "memory", "scc");
}
__device__ __forceinline__ void dma_x(unsigned v0, unsigned v1, unsigned vh, const void* sbase, unsigned lds, unsigned ldh) {
  unsigned sv;
  asm volatile("s_mov_b32 %0, m0\n\ts_mov_b32 m0, %5\n\ts_nop 0\n\tglobal_load_lds_dwordx4 %1, %4\n\t"
               "s_add_u32 m0, m0, 0x400\n\ts_nop 0\n\tglobal_load_lds_dwordx4 %2, %4\n\t"
               "s_mov_b32 m0, %6\n\ts_mov_b32 exec_hi, 0\n\ts_nop 0\n\tglobal_load_lds_dwordx4 %3, %4\n\ts_mov_b32 exec_hi, -1\n\ts_mov_b32 m0, %0"
               : "=&s"(sv) : "v"(v0), "v"(v1), "v"(vh), "s"(sbase), "s"(lds), "s"(ldh) : "memory", "scc");
}
template <int OFF> __device__ __forceinline__ u32x4 gld16(unsigned voff, const void* sbase) {
  u32x4 r;
  asm volatile("global_load_dwordx4 %0, %1, %2 offset:%3" : "=v"(r) : "v"(voff), "s"(sbase), "n"(OFF) : "memory");
  return r;
}
template <int OFF> __device__ __forceinline__ u32x4 gld16_sc1(unsigned voff, const void* sbase) {
  u32x4 r;
  asm volatile("global_load_dwordx4 %0, %1, %2 offset:%3 sc1" : "=v"(r) : "v"(voff), "s"(sbase), "n"(OFF) : "memory");
  return r;
}
__device__ __forceinline__ void gst16_sc1(unsigned voff, void* sbase, u32x4 v) {
  asm volatile("global_store_dwordx4 %0, %1, %2 sc1\n\ts_nop 1" :: "v"(voff), "v"(v), "s"(sbase) : "memory");
}
__device__ __forceinline__ void gst8_sc1(unsigned voff, void* sbase, u32x2 v) {
  asm volatile("global_store_dwordx2 %0, %1, %2 sc1\n\ts_nop 1" :: "v"(voff), "v"(v), "s"(sbase) : "memory");
}
__device__ __forceinline__ void gst4_sc1(unsigned voff, void* sbase, unsigned v) {
  asm volatile("global_store_dword %0, %1, %2 sc1\n\ts_nop 1" :: "v"(voff), "v"(v), "s"(sbase) : "memory");
}
__device__ __forceinline__ void put_flag(const void* p, u64 tag) {
  unsigned a; u64 v;
  asm volatile("s_mov_b64 exec, 1\n\tv_mov_b32 %0, 0\n\tv_mov_b64 %1, %2\n\tglobal_store_dwordx2 %0, %1, %3 sc1\n\ts_mov_b64 exec, -1\n\ts_nop 1"
               : "=&v"(a), "=&v"(v) : "s"(tag), "s"(p) : "memory");
}
__device__ __forceinline__ void poll_flags(unsigned voff, const void* sbase, u64 tag) {
#pragma unroll 1
  for (int it = 0; it < (1 << 20); ++it) {
    u64 f;
    asm volatile("global_load_dwordx2 %0, %1, %2 sc1\n\ts_waitcnt vmcnt(0)" : "=&v"(f) : "v"(voff), "s"(sbase) : "memory");
    if (__builtin_amdgcn_ballot_w64(f != tag) == 0) break;
    __builtin_amdgcn_s_sleep(2);
  }
}
__device__ __forceinline__ bf16x8 lds_b128(lds_cptr p) { return *(const LDS_AS bf16x8*)p; }
__device__ __forceinline__ s16x4 lds_tr(lds_cptr p) {
  return __builtin_amdgcn_ds_read_tr16_b64_v4i16((LDS_AS s16x4*)p);
}
__device__ __forceinline__ bf16x8 tr_frag(lds_cptr p, int off2) {
  s16x4 a = lds_tr(p), b = lds_tr(p + off2);
  s16x8 r = __builtin_shufflevector(a, b, 0, 1, 2, 3, 4, 5, 6, 7);
  return __builtin_bit_cast(bf16x8, r);
}
__device__ __forceinline__ unsigned pack2(float a, float b) {
  return __builtin_bit_cast(unsigned, __builtin_convertvector(f32x2{a, b}, bf16x2));
}
__device__ __forceinline__ s16x4 pack4(f32x4 a) {
  u32x2 r = {pack2(a[0], a[1]), pack2(a[2], a[3])};
  asm("" : "+v"(r));
  return __builtin_bit_cast(s16x4, r);
}
__device__ __forceinline__ float fmx(float a, float b) { return __builtin_elementwise_maximum(a, b); }
__device__ __forceinline__ u32x4 join16(s16x4 a, s16x4 b) {
  u32x2 x = __builtin_bit_cast(u32x2, a), y = __builtin_bit_cast(u32x2, b);
  auto r0 = __builtin_amdgcn_permlane16_swap(x[0], y[0], false, false);
  auto r1 = __builtin_amdgcn_permlane16_swap(x[1], y[1], false, false);
  return u32x4{r0[0], r1[0], r0[1], r1[1]};
}
__device__ __forceinline__ float xmax32(float v) {
  auto r = __builtin_amdgcn_permlane32_swap(__float_as_uint(v), __float_as_uint(v), false, false);
  return fmx(__uint_as_float(r[0]), __uint_as_float(r[1]));
}
__device__ __forceinline__ float xmax16(float v) {
  auto r = __builtin_amdgcn_permlane16_swap(__float_as_uint(v), __float_as_uint(v), false, false);
  return fmx(__uint_as_float(r[0]), __uint_as_float(r[1]));
}
__device__ __forceinline__ float max16(const f32x16& s) {
  float a = fmx(fmx(s[0], s[1]), s[2]);
#pragma unroll
  for (int i = 3; i < 15; i += 2) a = fmx(fmx(a, s[i]), s[i + 1]);
  return fmx(a, s[15]);
}

#define EX(r) "v_exp_f32 v" STR(r) ", v" STR(r) "\n\t"
#define E3(a, b, c) EX(a) EX(b) EX(c)
#define E5(a, b, c, d, e) EX(a) EX(b) EX(c) EX(d) EX(e)
#define SM(V, S) "v_smfmac_f32_32x32x32_bf16 v[112:127], " V ", " S ", v60\n\t"
#define MF(S, KOFF) "s_waitcnt lgkmcnt(0)\n\tv_mfma_f32_32x32x16_bf16 " S ", v[44:47], v[40:43], v[64:79]\n\tds_read_b128 v[44:47], v61 offset:" STR(KOFF) "\n\t"
#define VX(V1, O0, O1) "s_mov_b64 exec, %[mask]\n\tds_read_b128 v[48:51], v61 offset:" STR(O0) "\n\tds_read_b128 " V1 ", v61 offset:" STR(O1) "\n\ts_mov_b64 exec, -1\n\t"
#define PRIO 1
#define SP(p) "s_setprio " STR(p) "\n\t"
#define T32E(P, KOFF, O0, O1) E3(80, 81, 82) SP(P) SM("v[56:59]", "v[104:111]") E5(83, 84, 85, 86, 87) MF("v[96:111]", KOFF) \
  E5(88, 89, 90, 91, 92) SM("v[48:51]", "v[80:87]") VX("v[56:59]", O0, O1) E3(93, 94, 95)
#define T32O(P, KOFF, O0, O1) E3(96, 97, 98) SP(P) SM("v[52:55]", "v[88:95]") E5(99, 100, 101, 102, 103) MF("v[80:95]", KOFF) \
  E5(104, 105, 106, 107, 108) SM("v[48:51]", "v[96:103]") VX("v[52:55]", O0, O1) E3(109, 110, 111)
constexpr u64 VMASK = 0x0000FFFF0000FFFFull;

#define CL4(a, b, c, d) "v" #a, "v" #b, "v" #c, "v" #d
#define CL10(a) "v" STR(a##0), "v" STR(a##1), "v" STR(a##2), "v" STR(a##3), "v" STR(a##4), "v" STR(a##5), "v" STR(a##6), "v" STR(a##7), "v" STR(a##8), "v" STR(a##9)
__device__ __forceinline__ void att_loop(u32x4 q, unsigned ones, unsigned addr, unsigned gaddr, f32x16& oo, float& gate, const void* ws) {
  unsigned cnt = 8;
  asm volatile(
      "v_mov_b32 v48, %[ones]\n\tv_mov_b32 v49, %[ones]\n\tv_mov_b32 v60, 0xdddd\n\t"
      "v_mov_b64 v[50:51], v[48:49]\n\tv_mov_b64 v[52:53], v[48:49]\n\tv_mov_b64 v[54:55], v[48:49]\n\tv_mov_b64 v[56:57], v[48:49]\n\tv_mov_b64 v[58:59], v[48:49]\n\t"
      ".set zr%=, 104\n\t.rept 12\n\tv_mov_b64 v[zr%=:zr%=+1], 0\n\t.set zr%=, zr%=+2\n\t.endr\n\t"
      "s_barrier\n\t"
      "ds_read_b128 v[96:99], v61\n\t"
      "ds_read_b128 v[44:47], v61 offset:2048\n\t"
      VX("v[52:55]", 1024, 1280)
      "s_waitcnt lgkmcnt(3)\n\t"
      "v_mfma_f32_32x32x16_bf16 v[80:95], v[96:99], v[40:43], 0\n\t"
      "s_nop 15\n\t"
      "v_max3_f32 v64, v80, v81, v82\n\tv_max3_f32 v65, v83, v84, v85\n\tv_max3_f32 v66, v86, v87, v88\n\t"
      "v_max3_f32 v67, v89, v90, v91\n\tv_max3_f32 v68, v92, v93, v94\n\tv_max3_f32 v64, v64, v65, v95\n\t"
      "v_max3_f32 v66, v66, v67, v68\n\tv_max_f32 v64, v64, v66\n\tv_mov_b32 v65, v64\n\t"
      "s_nop 1\n\t"
      "v_permlane32_swap_b32 v64, v65\n\t"
      "s_nop 1\n\t"
      "v_min_f32_e64 v64, -v64, -v65\n\t"
      "v_mov_b32 v65, v64\n\t"
      "v_mov_b64 v[66:67], v[64:65]\n\tv_mov_b64 v[68:69], v[64:65]\n\tv_mov_b64 v[70:71], v[64:65]\n\tv_mov_b64 v[72:73], v[64:65]\n\t"
      "v_mov_b64 v[74:75], v[64:65]\n\tv_mov_b64 v[76:77], v[64:65]\n\tv_mov_b64 v[78:79], v[64:65]\n\t"
      "s_nop 1\n\t"
      "v_mfma_f32_32x32x16_bf16 v[80:95], v[96:99], v[40:43], v[64:79]\n\t"
      "s_nop 15\n\t"
      "LOOP%=:\n\t"
      T32E(3, 4096, 3072, 3328) T32O(2, 6144, 5120, 5376) T32E(1, 8192, 7168, 7424) T32O(0, 10240, 9216, 9472)
      T32E(3, 12288, 11264, 11520) T32O(2, 14336, 13312, 13568) T32E(1, 16384, 15360, 15616) T32O(0, 18432, 17408, 17664)
      "v_add_u32 v61, 0x4000, v61\n\t"
      "s_sub_u32 %[cnt], %[cnt], 1\n\t"
      "s_bitcmp1_b32 0xd4, %[cnt]\n\t"
      "s_cbranch_scc0 NB%=\n\t"
      "s_waitcnt vmcnt(0)\n\t"
      "s_barrier\n\t"
      "global_load_dword v63, v62, %[ws] sc1\n\t"
      "NB%=:\n\t"
      "s_cmp_lg_u32 %[cnt], 0\n\t"
      "s_cbranch_scc1 LOOP%=\n\t"
      SM("v[56:59]", "v[104:111]")
      "s_waitcnt vmcnt(0) lgkmcnt(0)\n\t"
      "s_nop 15\n\ts_nop 7\n\t"
      : "+{v61}"(addr), "=&{v63}"(gate), "=&{v[112:127]}"(oo), [cnt] "+s"(cnt)
      : "{v[40:43]}"(q), "{v62}"(gaddr), [ones] "v"(ones), [mask] "s"(VMASK), [ws] "s"(ws)
      : "memory", "scc", CL4(44, 45, 46, 47), "v48", "v49", CL10(5), "v60", CL4(64, 65, 66, 67), "v68", "v69", CL10(7), CL10(8), CL10(9), CL10(10), "v110", "v111");
}

constexpr int L_XR = 24576, L_WR = 55296, L_PT = 153600;
constexpr int L_WOB = 32768, L_SP = 131072;

struct Frag { bf16x8 w[4], x[2], h; };

__global__ __launch_bounds__(1024, 4) void k_fused(const u16* __restrict__ x, const u16* __restrict__ wqkv,
                                                   const u16* __restrict__ woa, const u16* __restrict__ wq,
                                                   const u16* __restrict__ wk, const u16* __restrict__ wv,
                                                   const u16* __restrict__ wob, const u16* __restrict__ gw,
                                                   char* ws, u16* __restrict__ out) {
  __shared__ __attribute__((aligned(1024))) char smem[163840];
  const unsigned sb = (unsigned)(size_t)(LDS_AS char*)smem;
  lds_cptr S = (lds_cptr)smem;
  lds_ptr SW = (lds_ptr)smem;
  const int b = blockIdx.x, xcd = b & 7, jj = b >> 3;
  const int tb = 8 * (xcd >> 1) + (jj >> 2), hg = 4 * (xcd & 1) + (jj & 3);
  const int T0 = 64 * tb;
  const int w = __builtin_amdgcn_readfirstlane(threadIdx.x >> 6);
#define LANE_VARS int l = threadIdx.x; asm volatile("" : "+v"(l)); l &= 63; \
  const int g = l >> 4, t = l & 15, q4 = t >> 2, p4 = l & 3, fsw = ((q4 >> 1) & 1) | ((g & 1) << 1)

  if (w >= 8) {
    LANE_VARS;
    const int hd = w & 3, tt = (w >> 2) & 1, n = l & 31;
    const int wa = L_WR + (8 * g + q4) * 128 + ((hd ^ fsw) * 32) + p4 * 8;
    const int xa = L_XR + (32 * tt + t) * 128 + ((g ^ ((t >> 1) & 7)) * 16);
    const int ha = L_XR + 8192 + (8 * tt + (t & 7)) * 128 + ((g ^ (((t >> 1) & 3) << 1)) * 16);
    f32x4 aq[2] = {}, aa[2] = {}, ak[2] = {}, av[2] = {}, akh = {}, avh = {};
    f32x4 mo, mp, mp0;
#pragma unroll
    for (int r = 0; r < 4; ++r) {
      mo[r] = ((unsigned)(t - 4 * g - r) <= 7u) ? 0.f : -__builtin_inff();
      mp[r] = (4 * g + r >= t + 9) ? 0.f : -__builtin_inff();
      mp0[r] = tb > 0 ? mp[r] : -__builtin_inff();
    }
    asm volatile("" : "+v"(mo), "+v"(mp), "+v"(mp0));
    int wo = 0, xo = 0;
    auto load = [&](Frag& f, int ks) {
      lds_cptr wp = S + wa + wo + ks * 16384;
#pragma unroll
      for (int m = 0; m < 4; ++m) f.w[m] = tr_frag(wp + m * 4096, 512);
      f.x[0] = lds_b128(S + (xa ^ (ks * 64)) + xo); f.x[1] = lds_b128(S + (xa ^ (ks * 64)) + xo + 2048);
      f.h = lds_b128(S + (ha ^ (ks * 64)) + xo);
    };
    auto mma = [&](const Frag& f) {
#pragma unroll
      for (int i = 0; i < 2; ++i) {
        aq[i] = MFMA16(f.w[0], f.x[i], aq[i]); aa[i] = MFMA16(f.w[1], f.x[i], aa[i]);
        ak[i] = MFMA16(f.w[2], f.x[i], ak[i]); av[i] = MFMA16(f.x[i], f.w[3], av[i]);
      }
      akh = MFMA16(f.w[2], f.h, akh); avh = MFMA16(f.h, f.w[3], avh);
    };
    auto mix = [&]() {
#pragma unroll
      for (int i = 0; i < 4; ++i) {
        __builtin_amdgcn_sched_group_barrier(0x008, 1, 0);
        __builtin_amdgcn_sched_group_barrier(0x100, 3, 0);
      }
      __builtin_amdgcn_sched_group_barrier(0x008, 6, 0);
    };
    Frag f0, f1;
    BAR(); BAR();

    load(f0, 0);
    LBAR();
    load(f1, 1); mma(f0); mix();
#pragma unroll 1
    for (int j = 1; j < 8; ++j) {
      wo = wo == 65536 ? 0 : wo + 32768; xo = xo == 20480 ? 0 : xo + 10240;
      LBAR();
      load(f0, 0); mma(f1); mix();
      LBAR();
      load(f1, 1); mma(f0); mix();
    }
    mma(f1);

    {
      s16x4 kk[3] = {pack4(akh), pack4(ak[0]), pack4(ak[1])}, vv[3] = {pack4(avh), pack4(av[0]), pack4(av[1])};
      const s16x4 on = {0x3f80, 0x3f80, 0x3f80, 0x3f80};
      s16x4 ob[2], po[2], pp[2];
      f32x4 so[2], sp[2], o[2], ls[2];
      float m[2];
      const f32x4 z = {0, 0, 0, 0};
#pragma unroll
      for (int i = 0; i < 2; ++i) {
        s16x4 q = pack4(aa[i] * QSCALE);
        so[i] = MFMA16K(kk[i + 1], q, mo); sp[i] = MFMA16K(kk[i], q, i ? mp : mp0);
      }
#pragma unroll
      for (int i = 0; i < 2; ++i) m[i] = fmx(fmx(fmx(fmx(so[i][0], so[i][1]), so[i][2]), fmx(fmx(so[i][3], sp[i][0]), sp[i][1])), fmx(sp[i][2], sp[i][3]));
      {
        auto r = __builtin_amdgcn_permlane16_swap(__float_as_uint(m[0]), __float_as_uint(m[1]), false, false);
        float y = xmax32(fmx(__uint_as_float(r[0]), __uint_as_float(r[1])));
        auto u = __builtin_amdgcn_permlane16_swap(__float_as_uint(y), __float_as_uint(y), false, false);
        m[0] = __uint_as_float(u[0]); m[1] = __uint_as_float(u[1]);
      }
#pragma unroll
      for (int i = 0; i < 2; ++i) {
#pragma unroll
        for (int r = 0; r < 4; ++r) { so[i][r] = __builtin_amdgcn_exp2f(so[i][r] - m[i]); sp[i][r] = __builtin_amdgcn_exp2f(sp[i][r] - m[i]); }
        po[i] = pack4(so[i]); pp[i] = pack4(sp[i]);
        ls[i] = MFMA16K(on, pp[i], MFMA16K(on, po[i], z));
        o[i] = MFMA16K(vv[i], pp[i], MFMA16K(vv[i + 1], po[i], z));
      }
#pragma unroll
      for (int i = 0; i < 2; ++i) ob[i] = pack4(o[i] * __builtin_amdgcn_rcpf(ls[i][0]));
      gst16_sc1(WS_O + ((2 * tb + tt) * 64 + 4 * hg + hd) * 1024 + l * 16, ws, join16(ob[0], ob[1]));
    }
    const u32x4 qf = join16(pack4(aq[0] * QSCALE), pack4(aq[1] * QSCALE));
    const unsigned ones = ((l & 27) == 16) ? 0x3f803f80u : 0u;
    const u32x4 one4 = {ones, ones, ones, ones};
    f32x16 oo; float gate;

    att_loop(qf, ones, sb + l * 16, WS_G + (T0 + 2 * n + tt) * 4, oo, gate, ws);

    bool ok = oo[8] < 1.2676506e30f;
#pragma unroll
    for (int i = 0; i < 8; ++i) ok = ok && (fabsf(oo[i]) < 3.0e38f);
    if (__builtin_amdgcn_ballot_w64(!ok) != 0) {
      const bf16x8 qq = __builtin_bit_cast(bf16x8, qf);
      float m = -__builtin_inff();
#pragma unroll 1
      for (int kt = 0; kt < 64; ++kt) {
        bf16x8 kf;
        asm volatile("global_load_dwordx4 %0, %1, %2 sc1\n\ts_waitcnt vmcnt(0)" : "=&v"(kf) : "v"(WS_KV + kt * 2048 + l * 16), "s"(ws) : "memory");
        m = fmx(m, max16(MFMA32(kf, qq, f32x16{0})));
      }
      m = xmax32(m);
      oo = f32x16{0};
#pragma unroll 1
      for (int kt = 0; kt < 64; ++kt) {
        bf16x8 kf; u32x4 v0, v1;
        asm volatile("global_load_dwordx4 %0, %3, %4 sc1\n\tglobal_load_dwordx4 %1, %3, %4 offset:1024 sc1\n\t"
                     "global_load_dwordx4 %2, %3, %4 offset:1280 sc1\n\ts_waitcnt vmcnt(0)"
                     : "=&v"(kf), "=&v"(v0), "=&v"(v1) : "v"(WS_KV + kt * 2048 + l * 16), "s"(ws) : "memory");
        f32x16 s = MFMA32(kf, qq, f32x16{0});
        f32x8 p0, p1;
#pragma unroll
        for (int i = 0; i < 8; ++i) { p0[i] = __builtin_amdgcn_exp2f(s[i] - m); p1[i] = __builtin_amdgcn_exp2f(s[8 + i] - m); }
        if (n >= 16) { v0 = one4; v1 = one4; }
        oo = __builtin_amdgcn_smfmac_f32_32x32x32_bf16(__builtin_bit_cast(bf16x8, v0), __builtin_bit_cast(bf16x16, p0), oo, 0xdddd, 0, 0);
        oo = __builtin_amdgcn_smfmac_f32_32x32x32_bf16(__builtin_bit_cast(bf16x8, v1), __builtin_bit_cast(bf16x16, p1), oo, 0xdddd, 0, 0);
      }
    }
    {
      float f = (1.f - gate) * __builtin_amdgcn_rcpf(oo[8]);
      bf16x8 ob;
#pragma unroll
      for (int j = 0; j < 8; ++j) ob[j] = (__bf16)(oo[j] * f);
      gst16_sc1(WS_O + ((2 * tb + tt) * 64 + 32 + 4 * hg + hd) * 1024 + l * 16, ws, __builtin_bit_cast(u32x4, ob));
    }
    WAIT_BAR(0);

    return;
  }

  __builtin_amdgcn_s_setprio(3);
  u64 tag = 0;
#define WVO (((l & 7) ^ ((g & 1) << 1)) * 16)
  if (w < 4) {
    LANE_VARS;
    u32x2 ep;
    asm volatile("global_load_dwordx2 %0, %1, %2 sc1" : "=v"(ep) : "v"(0u), "s"(ws + WS_EPOCH) : "memory");
    const int tk = T0 + 16 * (hg >> 1);
    const unsigned kxo = (unsigned)((tk + t) * 1024 + 256 * w + 16 * g);
    u32x4 kx0 = gld16<0>(kxo, x), kx1 = gld16<64>(kxo, x), kx2 = gld16<128>(kxo, x), kx3 = gld16<192>(kxo, x);
    dma4(l * 16, l * 16 + 1024, l * 16 + 2048, l * 16 + 3072, (const char*)((hg & 1) ? wv : wk) + 4096 * w, sb + 4096 * w);
    const unsigned gwo = 256 * w + 16 * g;
    u32x4 g0 = gld16<0>(gwo, gw), g1 = gld16<64>(gwo, gw), g2 = gld16<128>(gwo, gw), g3 = gld16<192>(gwo, gw);
    asm volatile("s_waitcnt vmcnt(0)" : "+v"(ep), "+v"(kx0), "+v"(kx1), "+v"(kx2), "+v"(kx3), "+v"(g0), "+v"(g1), "+v"(g2), "+v"(g3) :: "memory");

    tag = (((u64)(unsigned)__builtin_amdgcn_readfirstlane(ep[1]) << 32) | (unsigned)__builtin_amdgcn_readfirstlane(ep[0])) * 6364136223846793005ull + 1442695040888963407ull;
    f32x4 d = {0, 0, 0, 0}, dg = {0, 0, 0, 0};
    {
      lds_cptr wp = S + 4096 * w + (8 * g + q4) * 32 + p4 * 8;
      const bf16x8 xa[4] = {__builtin_bit_cast(bf16x8, kx0), __builtin_bit_cast(bf16x8, kx1), __builtin_bit_cast(bf16x8, kx2), __builtin_bit_cast(bf16x8, kx3)};
      const bf16x8 ga[4] = {__builtin_bit_cast(bf16x8, g0), __builtin_bit_cast(bf16x8, g1), __builtin_bit_cast(bf16x8, g2), __builtin_bit_cast(bf16x8, g3)};
      bf16x8 wf[4];
#pragma unroll
      for (int i = 0; i < 4; ++i) wf[i] = tr_frag(wp + 1024 * i, 128);
      if (hg & 1) {
#pragma unroll
        for (int i = 0; i < 4; ++i) { dg = MFMA16(ga[i], xa[i], dg); d = MFMA16(xa[i], wf[i], d); }
      } else {
#pragma unroll
        for (int i = 0; i < 4; ++i) d = MFMA16(wf[i], xa[i], d);
      }
    }
    if (w) {
      *(LDS_AS f32x4*)(SW + L_PT + 1280 * w + l * 16) = d;
      *(LDS_AS float*)(SW + L_PT + 1280 * w + 1024 + l * 4) = dg[0];
    }

    LBAR();

    if (w == 0) {
      const int sblk = (hg >> 1) & 1;
      const int tp = (t & 3) | ((t & 4) << 1) | ((t & 8) >> 1);
      unsigned o = WS_KV + (tk >> 5) * 2048 + (g >> 1) * 512 + sblk * 256 + (g & 1) * 8 + ((hg & 1) ? 1024 + tp * 16 : t * 16);
      float dz = dg[0];
#pragma unroll
      for (int i = 1; i < 4; ++i) { d += *(const LDS_AS f32x4*)(S + L_PT + 1280 * i + l * 16); dz += *(const LDS_AS float*)(S + L_PT + 1280 * i + 1024 + l * 4); }
      gst8_sc1(o, ws, __builtin_bit_cast(u32x2, pack4(d)));
      if ((hg & 1) && l < 16) gst4_sc1(WS_G + (tk + l) * 4, ws, __float_as_uint(__builtin_amdgcn_rcpf(1.f + __expf(-dz))));
    }

    BAR(); BAR();

    if (w == 0) {
      asm volatile("s_waitcnt vmcnt(0)" ::: "memory");
      put_flag(ws + WS_FKV + (tk >> 5) * 256 + (hg & 3) * 8, tag);
    }

    {
      const int X = xcd >> 1;
      unsigned toff = X * 32768, spin = 1 << 18, late = 0;
      unsigned vld = sb + w * 2048 + l * 16;
#define KV_LD(N, R) ".set kvr%=, " STR(R) "\n\t.rept " STR(N) "\n\t" \
          "v_add_u32 v23, %[toff], %[vkv]\n\t" \
          "global_load_dwordx4 v[kvr%=:kvr%=+3], v23, %[ws] sc1\n\t" \
          "global_load_dwordx4 v[kvr%=+4:kvr%=+7], v23, %[ws] offset:1024 sc1\n\t" \
          "s_add_u32 %[toff], %[toff], 0x2000\n\ts_and_b32 %[toff], %[toff], 0x1ffff\n\t" \
          ".set kvr%=, kvr%=+8\n\t.endr\n\t"
#define KV_ST(N, R, C) ".set kvr%=, " STR(R) "\n\t.set kvn%=, " STR(C) "\n\t.rept " STR(N) "\n\t" \
          "s_waitcnt vmcnt(kvn%=)\n\t" \
          "ds_write_b128 %[vld], v[kvr%=:kvr%=+3]\n\t" \
          "ds_write_b128 %[vld], v[kvr%=+4:kvr%=+7] offset:1024\n\t" \
          "v_add_u32 %[vld], 0x2000, %[vld]\n\t" \
          ".set kvr%=, kvr%=+8\n\t.set kvn%=, kvn%=-2\n\t.endr\n\t"
      asm volatile(
          ".rept 7\n\ts_barrier\n\t.endr\n\t"
          "global_load_dwordx2 v[24:25], %[vfl], %[ws] sc1\n\t"
          "s_barrier\n\ts_barrier\n\t"
          "s_waitcnt vmcnt(0)\n\t"
          "v_cmp_ne_u64 vcc, %[tag], v[24:25]\n\t"
          "s_cmp_eq_u64 vcc, 0\n\t"
          "s_cbranch_scc1 GO%=\n\t"
          ".rept 5\n\ts_barrier\n\t.endr\n\t"
          "s_mov_b32 %[late], 1\n\t"
          "PL%=:\n\t"
          "global_load_dwordx2 v[24:25], %[vfl], %[ws] sc1\n\t"
          "s_waitcnt vmcnt(0)\n\t"
          "v_cmp_ne_u64 vcc, %[tag], v[24:25]\n\t"
          "s_cmp_eq_u64 vcc, 0\n\t"
          "s_cbranch_scc1 GO%=\n\t"
          "s_sleep 8\n\t"
          "s_sub_u32 %[spin], %[spin], 1\n\t"
          "s_cbranch_scc0 PL%=\n\t"
          "GO%=:\n\t"
          KV_LD(3, 24)
          "s_cmp_lg_u32 %[late], 0\n\ts_cbranch_scc1 N1%=\n\t"
          "s_barrier\n\t"
          "N1%=:\n\t"
          KV_LD(10, 48)
          "s_cmp_lg_u32 %[late], 0\n\ts_cbranch_scc1 N2%=\n\t"
          ".rept 4\n\ts_barrier\n\t.endr\n\t"
          "N2%=:\n\t"
          KV_ST(3, 24, 24)
          "s_waitcnt lgkmcnt(0)\n\t"
          KV_LD(3, 24)
          "s_barrier\n\t"
          KV_ST(10, 48, 24)
          KV_ST(3, 24, 4)
          : [toff] "+s"(toff), [vld] "+v"(vld), [spin] "+s"(spin), [late] "+s"(late)
          : [vkv] "v"(WS_KV + w * 2048 + l * 16), [ws] "s"(ws), [tag] "s"(tag),
            [vfl] "v"(WS_FKV + ((16 * X + (l & 60) + w) & 63) * 256 + p4 * 8)
          : "memory", "scc", "vcc", "v23", CL4(24, 25, 26, 27), "v28", "v29", CL10(3), CL10(4), CL10(5), CL10(6), CL10(7), CL10(8), CL10(9), CL10(10), CL10(11),
            CL4(120, 121, 122, 123), CL4(124, 125, 126, 127));
    }

    LBAR();
    if (b == 0 && w == 0) put_flag(ws + WS_EPOCH, tag);
  } else {
    LANE_VARS;
    const int m = w - 4;
    const int ld2 = (m == 0) ? 1024 : 3072;
    const char* wsrc = (m == 0 ? (const char*)wq : (const char*)wqkv + (m - 1) * 1024) + 128 * hg;
    const unsigned w0 = (l >> 3) * ld2 + WVO, w1 = (w0 ^ 64) + 8 * ld2, w2 = w0 + 16 * ld2, w3 = w1 + 16 * ld2;
    const int r0 = 16 * m + (l >> 3);
    const unsigned x0 = (T0 + 2 * (r0 & 31) + (r0 >> 5)) * 1024 + (((l & 7) ^ ((r0 >> 1) & 7)) * 16);
    const unsigned x1 = (x0 ^ 64) + 16 * 1024;
    const int rh = 4 * m + ((l >> 3) & 3);
    const int th = T0 - 16 + 2 * (rh & 7) + (rh >> 3);
    const unsigned xh = (th < 0 ? 0 : th) * 1024 + (((l & 7) ^ (((rh >> 1) & 3) << 1)) * 16);
    const int rot = (tb + 2 * (jj & 3)) & 7;
    int wk_ = 2 * rot, wo = 0, xk = rot, xo = 0;
    auto stage_w = [&]() {
      dma4(w0, w1, w2, w3, wsrc + (size_t)wk_ * 32 * ld2, sb + L_WR + wo + m * 4096);
      wk_ = (wk_ + 1) & 15; wo = wo == 81920 ? 0 : wo + 16384;
    };
    auto stage_x = [&]() {
      dma_x(x0, x1, xh, (const char*)x + xk * 128, sb + L_XR + xo + m * 2048, sb + L_XR + xo + 8192 + m * 512);
      xk = (xk + 1) & 7; xo = xo == 20480 ? 0 : xo + 10240;
    };

    stage_x(); stage_w(); stage_w(); stage_x(); stage_w();

    BAR();
    stage_w(); stage_x(); stage_w(); stage_w();

    WAIT_BAR(26);

    WAIT_BAR(22);

#pragma unroll 1
    for (int s = 0; s < 5; ++s) {
      stage_w();
      WAIT_BAR(19);

      stage_x(); stage_w();
      WAIT_BAR(22);

    }
    WAIT_BAR(15); WAIT_BAR(11); WAIT_BAR(4); WAIT_BAR(0);

    BAR();

    BAR();
  }

  LANE_VARS;
  const int tbo = 4 * xcd + (jj >> 3), cg = jj & 7;
  const int to = (w >> 1) & 1, hf = w & 1, kh = w >> 2;
  unsigned o0 = (l >> 3) * 1024 + WVO;
  const unsigned ovo = WS_O + ((2 * tbo + to) * 64 + 16 * kh + (g >> 1)) * 1024 + ((g & 1) * 32 + 16 * hf + t) * 16;
  const int tok = 64 * tbo + 2 * (16 * hf + t) + to;
  const unsigned oso = tok * 1024 + 128 * cg + 64 * kh + 32 * (g & 1) + 16 * (g >> 1);
  int wo = (8 * g + q4) * 128 + fsw * 32 + p4 * 8 + kh * L_SP, rb = 32768 + w * 2048 + l * 16;
  f32x4 acc[4] = {};
  unsigned go;
  const char* fl = ws + WS_FA;
  const char* ob = ws;
  auto stage = [&](const char* p, unsigned dst) {
    asm volatile("" : "+v"(o0));
    dma4(o0, (o0 ^ 64) + 8192, o0 + 16384, (o0 ^ 64) + 24576, p, dst);
  };
  auto signal = [&]() { if (w == 0) put_flag(fl + (tb * 8 + hg) * 256, tag); };
  signal();
  stage((const char*)woa + 128 * cg + 262144 + w * 32768, sb + L_SP + w * 4096);
  asm volatile("global_load_dword %0, %1, %2 sc1\n\ts_barrier" : "=v"(go) : "v"(WS_G + tok * 4), "s"(ws) : "memory");
  stage((const char*)woa + 128 * cg + w * 32768, sb + w * 4096);
  __builtin_amdgcn_s_sleep(32);
#pragma unroll 1
  for (int pass = 0;; ++pass) {
    asm volatile("" : "+s"(pass));
    if (w == 0) poll_flags(tbo * 2048 + ((rb << 4) & 0x700), fl, tag);
    asm volatile("s_waitcnt vmcnt(0)\n\ts_barrier" : "+v"(go) :: "memory");

    u32x4 of[8];
    of[0] = gld16_sc1<0>(ovo, ob); of[1] = gld16_sc1<2048>(ovo, ob); of[2] = gld16_sc1<0>(ovo, ob + 4096); of[3] = gld16_sc1<2048>(ovo, ob + 4096);
    of[4] = gld16_sc1<0>(ovo, ob + 8192); of[5] = gld16_sc1<2048>(ovo, ob + 8192); of[6] = gld16_sc1<0>(ovo, ob + 12288); of[7] = gld16_sc1<2048>(ovo, ob + 12288);
    bf16x8 wf[4][4];
#pragma unroll
    for (int j = 0; j < 4; ++j)
#pragma unroll
      for (int c = 0; c < 4; ++c) wf[j][c] = tr_frag(S + (wo ^ (32 * c)) + j * 4096, 512);
    asm volatile("s_waitcnt vmcnt(4)" : "+v"(of[0]), "+v"(of[1]), "+v"(of[2]), "+v"(of[3]) :: "memory");

#pragma unroll
    for (int j = 0; j < 4; ++j)
#pragma unroll
      for (int c = 0; c < 4; ++c) {
        acc[c] = MFMA16(wf[j][c], __builtin_bit_cast(bf16x8, of[j]), acc[c]);
        wf[j][c] = tr_frag(S + (wo ^ (32 * c)) + (4 + j) * 4096, 512);
      }
    asm volatile("s_waitcnt vmcnt(0)" : "+v"(of[4]), "+v"(of[5]), "+v"(of[6]), "+v"(of[7]) :: "memory");
#pragma unroll
    for (int j = 0; j < 4; ++j)
#pragma unroll
      for (int c = 0; c < 4; ++c) acc[c] = MFMA16(wf[j][c], __builtin_bit_cast(bf16x8, of[4 + j]), acc[c]);

    {
      *(LDS_AS f32x4*)(SW + rb) = kh ? acc[0] : acc[2];
      *(LDS_AS f32x4*)(SW + rb + 1024) = kh ? acc[1] : acc[3];
      LBAR();
      lds_cptr pr = S + (rb ^ 8192);
      f32x4 r0 = (kh ? acc[2] : acc[0]) + *(const LDS_AS f32x4*)(pr), r1 = (kh ? acc[3] : acc[1]) + *(const LDS_AS f32x4*)(pr + 1024);
      *(u32x4*)((char*)out + oso) = join16(pack4(r0), pack4(r1));
    }
    if (pass) break;

#pragma unroll
    for (int c = 0; c < 4; ++c) acc[c] *= __uint_as_float(go);
    const char* p = (const char*)wob + 128 * cg + w * 65536;
    stage(p, sb + L_WOB + w * 8192);
    stage(p + 32768, sb + L_WOB + w * 8192 + 4096);
    fl += WS_FB - WS_FA; ob += 32768; rb -= 32768; wo += L_WOB + kh * (32768 - L_SP);
    asm volatile("s_waitcnt vmcnt(0)\n\ts_barrier" : "+v"(acc[0]), "+v"(acc[1]), "+v"(acc[2]), "+v"(acc[3]), "+v"(wo), "+v"(rb) :: "memory");
    signal();

  }

}

extern "C" void kernel_launch(void* const* d_in, const int* in_sizes, int n_in, void* d_out, int out_size, void* d_ws,
                              size_t ws_size, hipStream_t stream) {
  k_fused<<<256, 1024, 0, stream>>>((const u16*)d_in[0], (const u16*)d_in[1], (const u16*)d_in[2], (const u16*)d_in[4],
                                    (const u16*)d_in[5], (const u16*)d_in[6], (const u16*)d_in[7], (const u16*)d_in[8],
                                    (char*)d_ws, (u16*)d_out);
}
```

```cpp
#include <hip/hip_runtime.h>
#include <hip/hip_bf16.h>

typedef unsigned short u16;
typedef unsigned long long u64;
typedef __attribute__((ext_vector_type(2))) float f32x2;
typedef __attribute__((ext_vector_type(2))) __bf16 bf16x2;
typedef __attribute__((ext_vector_type(4))) float f32x4;
typedef __attribute__((ext_vector_type(8))) float f32x8;
typedef __attribute__((ext_vector_type(16))) float f32x16;
typedef __attribute__((ext_vector_type(32))) float f32x32;
typedef __attribute__((ext_vector_type(16))) __bf16 bf16x16;
typedef __attribute__((ext_vector_type(8))) __bf16 bf16x8;
typedef __attribute__((ext_vector_type(4))) __bf16 bf16x4;
typedef __attribute__((ext_vector_type(4))) short s16x4;
typedef __attribute__((ext_vector_type(8))) short s16x8;
typedef __attribute__((ext_vector_type(4))) unsigned u32x4;
typedef __attribute__((ext_vector_type(2))) unsigned u32x2;
typedef __attribute__((ext_vector_type(8))) unsigned u32x8;
typedef __attribute__((ext_vector_type(16))) unsigned u32x16;
#define LDS_AS __attribute__((address_space(3)))
typedef LDS_AS const char* lds_cptr;
typedef LDS_AS char* lds_ptr;

#define MFMA32(a, b, c) __builtin_amdgcn_mfma_f32_32x32x16_bf16(a, b, c, 0, 0, 0)
#define MFMA16(a, b, c) __builtin_amdgcn_mfma_f32_16x16x32_bf16(a, b, c, 0, 0, 0)
#define MFMA16K(a, b, c) __builtin_amdgcn_mfma_f32_16x16x16bf16_1k(a, b, c, 0, 0, 0)
#define WAIT_BAR(N) asm volatile("s_waitcnt vmcnt(" #N ") lgkmcnt(0)\n\ts_barrier" ::: "memory")
#define LBAR() asm volatile("s_waitcnt lgkmcnt(0)\n\ts_barrier" ::: "memory")
#define BAR() asm volatile("s_barrier" ::: "memory")
#define STR2(x) #x
#define STR(x) STR2(x)

constexpr unsigned WS_EPOCH = 0;
constexpr unsigned WS_FKV = 4096;
constexpr unsigned WS_G = 32768;
constexpr unsigned WS_KV = 65536;
constexpr unsigned WS_FA = 262144;
constexpr unsigned WS_FB = 327680;
constexpr unsigned WS_KG = 524288;
constexpr unsigned WS_O = 1u << 20;

constexpr float QSCALE = 0.25f * 1.4426950408889634f;

__device__ __forceinline__ void dma4(unsigned v0, unsigned v1, unsigned v2, unsigned v3, const void* sbase, unsigned lds) {
  unsigned sv;
  asm volatile("s_mov_b32 %0, m0\n\ts_mov_b32 m0, %6\n\ts_nop 0\n\tglobal_load_lds_dwordx4 %1, %5\n\t"
               "s_add_u32 m0, m0, 0x400\n\ts_nop 0\n\tglobal_load_lds_dwordx4 %2, %5\n\t"
               "s_add_u32 m0, m0, 0x400\n\ts_nop 0\n\tglobal_load_lds_dwordx4 %3, %5\n\t"
               "s_add_u32 m0, m0, 0x400\n\ts_nop 0\n\tglobal_load_lds_dwordx4 %4, %5\n\ts_mov_b32 m0, %0"
               : "=&s"(sv) : "v"(v0), "v"(v1), "v"(v2), "v"(v3), "s"(sbase), "s"(lds) : "memory", "scc");
}
__device__ __forceinline__ void dma2(unsigned v0, const void* s0, unsigned l0, unsigned v1, const void* s1, unsigned l1) {
  unsigned sv;
  asm volatile("s_mov_b32 %0, m0\n\ts_mov_b32 m0, %3\n\ts_nop 0\n\tglobal_load_lds_dwordx4 %1, %2\n\t"
               "s_mov_b32 m0, %6\n\ts_nop 0\n\tglobal_load_lds_dwordx4 %4, %5\n\ts_mov_b32 m0, %0"
               : "=&s"(sv) : "v"(v0), "s"(s0), "s"(l0), "v"(v1), "s"(s1), "s"(l1) : "memory");
}
__device__ __forceinline__ void dma3(unsigned v0, const void* s0, unsigned l0) {
  unsigned sv;
  asm volatile("s_mov_b32 %0, m0\n\ts_mov_b32 m0, %3\n\ts_nop 0\n\tglobal_load_lds_dwordx4 %1, %2\n\t"
               "global_load_lds_dwordx4 %1, %2 offset:1024\n\tglobal_load_lds_dwordx4 %1, %2 offset:2048\n\ts_mov_b32 m0, %0"
               : "=&s"(sv) : "v"(v0), "s"(s0), "s"(l0) : "memory");
}
template <int OFF> __device__ __forceinline__ u32x4 gld16(unsigned voff, const void* sbase) {
  u32x4 r;
  asm volatile("global_load_dwordx4 %0, %1, %2 offset:%3" : "=v"(r) : "v"(voff), "s"(sbase), "n"(OFF) : "memory");
  return r;
}
template <int OFF> __device__ __forceinline__ u32x4 gld16_sc1(unsigned voff, const void* sbase) {
  u32x4 r;
  asm volatile("global_load_dwordx4 %0, %1, %2 offset:%3 sc1" : "=v"(r) : "v"(voff), "s"(sbase), "n"(OFF) : "memory");
  return r;
}
__device__ __forceinline__ void gst16_sc1(unsigned voff, void* sbase, u32x4 v) {
  asm volatile("global_store_dwordx4 %0, %1, %2 sc1\n\ts_nop 1" :: "v"(voff), "v"(v), "s"(sbase) : "memory");
}
__device__ __forceinline__ void gst8_sc1(unsigned voff, void* sbase, u32x2 v) {
  asm volatile("global_store_dwordx2 %0, %1, %2 sc1\n\ts_nop 1" :: "v"(voff), "v"(v), "s"(sbase) : "memory");
}
__device__ __forceinline__ void gst4_sc1(unsigned voff, void* sbase, unsigned v) {
  asm volatile("global_store_dword %0, %1, %2 sc1\n\ts_nop 1" :: "v"(voff), "v"(v), "s"(sbase) : "memory");
}
__device__ __forceinline__ void put_flag(const void* p, u64 tag) {
  unsigned a; u64 v;
  asm volatile("s_mov_b64 exec, 1\n\tv_mov_b32 %0, 0\n\tv_mov_b64 %1, %2\n\tglobal_store_dwordx2 %0, %1, %3 sc1\n\ts_mov_b64 exec, -1\n\ts_nop 1"
               : "=&v"(a), "=&v"(v) : "s"(tag), "s"(p) : "memory");
}
__device__ __forceinline__ void poll_flags(unsigned voff, const void* sbase, u64 tag) {
#pragma unroll 1
  for (int it = 0; it < (1 << 20); ++it) {
    u64 f;
    asm volatile("global_load_dwordx2 %0, %1, %2 sc1\n\ts_waitcnt vmcnt(0)" : "=&v"(f) : "v"(voff), "s"(sbase) : "memory");
    if (__builtin_amdgcn_ballot_w64(f != tag) == 0) break;
    __builtin_amdgcn_s_sleep(2);
  }
}
__device__ __forceinline__ bf16x8 lds_b128(lds_cptr p) { return *(const LDS_AS bf16x8*)p; }
__device__ __forceinline__ s16x4 lds_tr(lds_cptr p) {
  return __builtin_amdgcn_ds_read_tr16_b64_v4i16((LDS_AS s16x4*)p);
}
__device__ __forceinline__ bf16x8 tr_frag(lds_cptr p, int off2) {
  s16x4 a = lds_tr(p), b = lds_tr(p + off2);
  s16x8 r = __builtin_shufflevector(a, b, 0, 1, 2, 3, 4, 5, 6, 7);
  return __builtin_bit_cast(bf16x8, r);
}
__device__ __forceinline__ unsigned pack2(float a, float b) {
  return __builtin_bit_cast(unsigned, __builtin_convertvector(f32x2{a, b}, bf16x2));
}
__device__ __forceinline__ s16x4 pack4(f32x4 a) {
  u32x2 r = {pack2(a[0], a[1]), pack2(a[2], a[3])};
  asm("" : "+v"(r));
  return __builtin_bit_cast(s16x4, r);
}
__device__ __forceinline__ float fmx(float a, float b) { return __builtin_elementwise_maximum(a, b); }
__device__ __forceinline__ u32x4 join16(s16x4 a, s16x4 b) {
  u32x2 x = __builtin_bit_cast(u32x2, a), y = __builtin_bit_cast(u32x2, b);
  auto r0 = __builtin_amdgcn_permlane16_swap(x[0], y[0], false, false);
  auto r1 = __builtin_amdgcn_permlane16_swap(x[1], y[1], false, false);
  return u32x4{r0[0], r1[0], r0[1], r1[1]};
}
__device__ __forceinline__ float xmax32(float v) {
  auto r = __builtin_amdgcn_permlane32_swap(__float_as_uint(v), __float_as_uint(v), false, false);
  return fmx(__uint_as_float(r[0]), __uint_as_float(r[1]));
}
__device__ __forceinline__ float xmax16(float v) {
  auto r = __builtin_amdgcn_permlane16_swap(__float_as_uint(v), __float_as_uint(v), false, false);
  return fmx(__uint_as_float(r[0]), __uint_as_float(r[1]));
}
__device__ __forceinline__ float max16(const f32x16& s) {
  float a = fmx(fmx(s[0], s[1]), s[2]);
#pragma unroll
  for (int i = 3; i < 15; i += 2) a = fmx(fmx(a, s[i]), s[i + 1]);
  return fmx(a, s[15]);
}

constexpr int L_WQ = 65536, L_R0 = 147456;
constexpr int L_S2 = 114688;

#define EX(r) "v_exp_f32 v" STR(r) ", v" STR(r) "\n\t"
#define E3(a, b, c) EX(a) EX(b) EX(c)
#define E5(a, b, c, d, e) EX(a) EX(b) EX(c) EX(d) EX(e)
#define SM(V, S) "v_smfmac_f32_32x32x32_bf16 v[112:127], " V ", " S ", v60\n\t"
#define MF(S, KA, KOFF) "s_waitcnt lgkmcnt(0)\n\tv_mfma_f32_32x32x16_bf16 " S ", v[44:47], v[40:43], v[64:79]\n\tds_read_b128 v[44:47], " KA " offset:" STR(KOFF) "\n\t"
#define VX(V1, VA, O0, O1) "s_mov_b64 exec, %[mask]\n\tds_read_b128 v[48:51], " VA " offset:" STR(O0) "\n\tds_read_b128 " V1 ", " VA " offset:" STR(O1) "\n\ts_mov_b64 exec, -1\n\t"
#define SP(p) "s_setprio " STR(p) "\n\t"
#define T32E(P, KA, KOFF, VA, O0, O1) E3(80, 81, 82) SP(P) SM("v[56:59]", "v[104:111]") E5(83, 84, 85, 86, 87) MF("v[96:111]", KA, KOFF) \
  E5(88, 89, 90, 91, 92) SM("v[48:51]", "v[80:87]") VX("v[56:59]", VA, O0, O1) E3(93, 94, 95)
#define T32O(P, KA, KOFF, VA, O0, O1) E3(96, 97, 98) SP(P) SM("v[52:55]", "v[88:95]") E5(99, 100, 101, 102, 103) MF("v[80:95]", KA, KOFF) \
  E5(104, 105, 106, 107, 108) SM("v[48:51]", "v[96:103]") VX("v[52:55]", VA, O0, O1) E3(109, 110, 111)
constexpr u64 VMASK = 0x0000FFFF0000FFFFull;
#define P0 3
#define P1 2
#define P2 1
#define P3 0

#define CL4(a, b, c, d) "v" #a, "v" #b, "v" #c, "v" #d
#define CL10(a) "v" STR(a##0), "v" STR(a##1), "v" STR(a##2), "v" STR(a##3), "v" STR(a##4), "v" STR(a##5), "v" STR(a##6), "v" STR(a##7), "v" STR(a##8), "v" STR(a##9)
__device__ __forceinline__ void att_loop(u32x4 q, unsigned ones, unsigned lane, f32x16& oo, unsigned& c0) {
  unsigned cnt = 8, nx = L_R0 - 16384;
  asm volatile(
      "v_mov_b32 v48, %[ones]\n\tv_mov_b32 v49, %[ones]\n\tv_mov_b32 v60, 0xdddd\n\t"
      "v_add_u32 v61, 0x24000, v63\n\tv_add_u32 v62, 0x20000, v63\n\t"
      "v_mov_b64 v[50:51], v[48:49]\n\tv_mov_b64 v[52:53], v[48:49]\n\tv_mov_b64 v[54:55], v[48:49]\n\tv_mov_b64 v[56:57], v[48:49]\n\tv_mov_b64 v[58:59], v[48:49]\n\t"
      ".set zr%=, 104\n\t.rept 12\n\tv_mov_b64 v[zr%=:zr%=+1], 0\n\t.set zr%=, zr%=+2\n\t.endr\n\t"
      "s_barrier\n\t"
      "ds_read_b128 v[96:99], v61\n\t"
      "ds_read_b128 v[44:47], v61 offset:2048\n\t"
      VX("v[52:55]", "v61", 1024, 1280)
      "s_waitcnt lgkmcnt(3)\n\t"
      "v_mfma_f32_32x32x16_bf16 v[80:95], v[96:99], v[40:43], 0\n\t"
      "s_nop 15\n\t"
      "v_max3_f32 v64, v80, v81, v82\n\tv_max3_f32 v65, v83, v84, v85\n\tv_max3_f32 v66, v86, v87, v88\n\t"
      "v_max3_f32 v67, v89, v90, v91\n\tv_max3_f32 v68, v92, v93, v94\n\tv_max3_f32 v64, v64, v65, v95\n\t"
      "v_max3_f32 v66, v66, v67, v68\n\tv_max_f32 v64, v64, v66\n\tv_mov_b32 v65, v64\n\t"
      "s_nop 1\n\t"
      "v_permlane32_swap_b32 v64, v65\n\t"
      "s_nop 1\n\t"
      "v_min_f32_e64 v64, -v64, -v65\n\t"
      "v_mov_b32 v65, v64\n\t"
      "v_mov_b64 v[66:67], v[64:65]\n\tv_mov_b64 v[68:69], v[64:65]\n\tv_mov_b64 v[70:71], v[64:65]\n\tv_mov_b64 v[72:73], v[64:65]\n\t"
      "v_mov_b64 v[74:75], v[64:65]\n\tv_mov_b64 v[76:77], v[64:65]\n\tv_mov_b64 v[78:79], v[64:65]\n\t"
      "s_nop 1\n\t"
      "v_mfma_f32_32x32x16_bf16 v[80:95], v[96:99], v[40:43], v[64:79]\n\t"
      "s_nop 15\n\t"
      "LOOP%=:\n\t"
      T32E(P0, "v61", 4096, "v61", 3072, 3328) T32O(P1, "v61", 6144, "v61", 5120, 5376)
      T32E(P2, "v61", 8192, "v61", 7168, 7424) T32O(P3, "v61", 10240, "v61", 9216, 9472)
      T32E(P0, "v61", 12288, "v61", 11264, 11520)
      "ds_read_b32 %[c0], v61\n\t"
      T32O(P1, "v61", 14336, "v61", 13312, 13568)
      "s_barrier\n\t"
      T32E(P2, "v62", 0, "v61", 15360, 15616) T32O(P3, "v62", 2048, "v62", 1024, 1280)
      "v_mov_b32 v61, v62\n\t"
      "s_sub_u32 %[nx], %[nx], 0x4000\n\t"
      "s_cmp_lt_u32 %[nx], 0x1c000\n\t"
      "s_cselect_b32 %[nx], 0x24000, %[nx]\n\t"
      "s_sub_u32 %[cnt], %[cnt], 1\n\t"
      "v_add_u32 v62, %[nx], v63\n\t"
      "s_cmp_lg_u32 %[cnt], 0\n\t"
      "s_cbranch_scc1 LOOP%=\n\t"
      SM("v[56:59]", "v[104:111]")
      "s_waitcnt lgkmcnt(0)\n\t"
      "s_nop 15\n\ts_nop 7\n\t"
      : [c0] "=&v"(c0), "=&{v[112:127]}"(oo), [cnt] "+s"(cnt), [nx] "+s"(nx)
      : "{v[40:43]}"(q), "{v63}"(lane), [ones] "v"(ones), [mask] "s"(VMASK)
      : "memory", "scc", CL4(44, 45, 46, 47), "v48", "v49", CL10(5), "v60", "v61", "v62", CL4(64, 65, 66, 67), "v68", "v69", CL10(7), CL10(8), CL10(9), CL10(10), "v110", "v111");
}

#define NA0 2
#define NA1 5
#define ABAR 0xD5

__global__ __launch_bounds__(1024, 4) void k_fused(const u16* __restrict__ x, const u16* __restrict__ wqkv,
                                                   const u16* __restrict__ woa, const u16* __restrict__ wq,
                                                   const u16* __restrict__ wk, const u16* __restrict__ wv,
                                                   const u16* __restrict__ wob, const u16* __restrict__ gw,
                                                   char* ws, u16* __restrict__ out) {
  __shared__ __attribute__((aligned(1024))) char smem[163840];
  const unsigned sb = (unsigned)(size_t)(LDS_AS char*)smem;
  lds_cptr S = (lds_cptr)smem;
  lds_ptr SW = (lds_ptr)smem;
  const int b = blockIdx.x, xcd = b & 7, jj = b >> 3;
  const int tb = 8 * (xcd >> 1) + (jj >> 2), hg = 4 * (xcd & 1) + (jj & 3);
  const int T0 = 64 * tb;
  const int rot = (tb + 2 * (jj & 3)) & 7;
  const int w = __builtin_amdgcn_readfirstlane(threadIdx.x >> 6);
#define LANE_VARS int l = threadIdx.x; asm volatile("" : "+v"(l)); l &= 63; \
  const int g = l >> 4, t = l & 15, q4 = t >> 2, p4 = l & 3, fsw = ((q4 >> 1) & 1) | ((g & 1) << 1)
#define WVO (((l & 7) ^ ((g & 1) << 1)) * 16)
#define MK_TAG(ep) ((((u64)(unsigned)__builtin_amdgcn_readfirstlane(ep[1]) << 32) | (unsigned)__builtin_amdgcn_readfirstlane(ep[0])) * 6364136223846793005ull + 1442695040888963407ull)

  if (w >= 8) {
    LANE_VARS;
    const int hd = w & 3, tt = (w >> 2) & 1, n = l & 31, cw = w - 8;
    u32x2 ep;
    __builtin_amdgcn_s_setprio(2);
    {
      const int r = 8 * cw + (l >> 3);
      const unsigned xv = (T0 + 2 * (r & 31) + (r >> 5)) * 1024 + (((l & 7) ^ ((r >> 1) & 7)) * 16);
      const unsigned wv_ = r * 1024 + (WVO ^ ((cw & 1) * 64));
#pragma unroll
      for (int i = 0; i < 4; ++i) {
        const int kc = (rot + i) & 7;
        dma2(xv, (const char*)x + kc * 128, sb + i * 8192 + cw * 1024, wv_, (const char*)wq + 128 * hg + kc * 65536, sb + L_WQ + i * 8192 + cw * 1024);
      }
      asm volatile("global_load_dwordx2 %0, %1, %2 sc1" : "=v"(ep) : "v"(0u), "s"(ws + WS_EPOCH) : "memory");
    }

    const int wa = L_WQ + (8 * g + q4) * 128 + ((hd ^ fsw) * 32) + p4 * 8;
    const int xa = (32 * tt + t) * 128 + ((g ^ ((t >> 1) & 7)) * 16);
    f32x4 aq[2] = {};
    auto quarter = [&](int h) {
      bf16x8 wf[4], x0[4], x1[4];
#pragma unroll
      for (int i = 0; i < 4; ++i) {
        const int s = 4 * h + i;
        lds_cptr xp = S + (xa ^ ((s & 1) * 64)) + (s >> 1) * 8192;
        wf[i] = tr_frag(S + wa + s * 4096, 512); x0[i] = lds_b128(xp); x1[i] = lds_b128(xp + 2048);
      }
      __builtin_amdgcn_sched_barrier(0);
#pragma unroll
      for (int i = 0; i < 4; ++i) { aq[0] = MFMA16(wf[i], x0[i], aq[0]); aq[1] = MFMA16(wf[i], x1[i], aq[1]); }
    };
    WAIT_BAR(5);

    quarter(0);
    asm volatile("s_waitcnt vmcnt(0)\n\ts_barrier" : "+v"(ep) :: "memory");

    quarter(1);
    BAR();

    quarter(2);
    BAR();

    quarter(3);
    const u64 tag = MK_TAG(ep);
    const u32x4 qf = join16(pack4(aq[0] * QSCALE), pack4(aq[1] * QSCALE));
    const unsigned ones = ((l & 27) == 16) ? 0x3f803f80u : 0u;
    const u32x4 one4 = {ones, ones, ones, ones};
    f32x16 oo; unsigned c0;

    att_loop(qf, ones, sb + l * 16, oo, c0);

    bool ok = oo[8] < 1.2676506e30f;
#pragma unroll
    for (int i = 0; i < 8; ++i) ok = ok && (fabsf(oo[i]) < 3.0e38f);
    if (__builtin_amdgcn_ballot_w64(!ok) != 0) {
      const bf16x8 qq = __builtin_bit_cast(bf16x8, qf);
      float m = -__builtin_inff();
#pragma unroll 1
      for (int kt = 0; kt < 64; ++kt) {
        bf16x8 kf;
        asm volatile("global_load_dwordx4 %0, %1, %2 sc1\n\ts_waitcnt vmcnt(0)" : "=&v"(kf) : "v"(WS_KV + kt * 2048 + l * 16), "s"(ws) : "memory");
        m = fmx(m, max16(MFMA32(kf, qq, f32x16{0})));
      }
      m = xmax32(m);
      oo = f32x16{0};
#pragma unroll 1
      for (int kt = 0; kt < 64; ++kt) {
        bf16x8 kf; u32x4 v0, v1;
        asm volatile("global_load_dwordx4 %0, %3, %4 sc1\n\tglobal_load_dwordx4 %1, %3, %4 offset:1024 sc1\n\t"
                     "global_load_dwordx4 %2, %3, %4 offset:1280 sc1\n\ts_waitcnt vmcnt(0)"
                     : "=&v"(kf), "=&v"(v0), "=&v"(v1) : "v"(WS_KV + kt * 2048 + l * 16), "s"(ws) : "memory");
        f32x16 s = MFMA32(kf, qq, f32x16{0});
        f32x8 p0, p1;
#pragma unroll
        for (int i = 0; i < 8; ++i) { p0[i] = __builtin_amdgcn_exp2f(s[i] - m); p1[i] = __builtin_amdgcn_exp2f(s[8 + i] - m); }
        if (n >= 16) { v0 = one4; v1 = one4; }
        oo = __builtin_amdgcn_smfmac_f32_32x32x32_bf16(__builtin_bit_cast(bf16x8, v0), __builtin_bit_cast(bf16x16, p0), oo, 0xdddd, 0, 0);
        oo = __builtin_amdgcn_smfmac_f32_32x32x32_bf16(__builtin_bit_cast(bf16x8, v1), __builtin_bit_cast(bf16x16, p1), oo, 0xdddd, 0, 0);
      }
    }
    {
      float f = __builtin_amdgcn_rcpf(oo[8]);
      bf16x8 ob;
#pragma unroll
      for (int j = 0; j < 8; ++j) ob[j] = (__bf16)(oo[j] * f);
      gst16_sc1(WS_O + ((2 * tb + tt) * 64 + 32 + 4 * hg + hd) * 1024 + l * 16, ws, __builtin_bit_cast(u32x4, ob));
    }
    unsigned old;
    asm volatile("s_waitcnt vmcnt(0)\n\ts_mov_b64 exec, 1\n\tds_add_rtn_u32 %0, %1, %2\n\ts_waitcnt lgkmcnt(0)\n\ts_mov_b64 exec, -1"
                 : "=&v"(old) : "v"(sb + L_R0 - 16384), "v"(1u) : "memory");
    if (__builtin_amdgcn_readfirstlane(old - c0) == 7) put_flag(ws + WS_FB + (tb * 8 + hg) * 256, tag);

    return;
  }

  u64 tag = 0;
  const int tbo = 4 * xcd + (jj >> 3), cg = jj & 7;
  if (w < 4) {
    __builtin_amdgcn_s_setprio(3);
    LANE_VARS;
    u32x2 ep;
    const int tk = T0 + 16 * (hg >> 1);
    const unsigned kxo = (unsigned)((tk + t) * 1024 + 256 * w + 16 * g);
    u32x4 kx0 = gld16<0>(kxo, x), kx1 = gld16<64>(kxo, x), kx2 = gld16<128>(kxo, x), kx3 = gld16<192>(kxo, x);
    {
      const unsigned kv = (8 * ((l >> 3) & 3) + 4 * (l >> 5) + ((l >> 1) & 3)) * 32 + (l & 1) * 16;
      dma4(kv, kv + 1024, kv + 2048, kv + 3072, (const char*)((hg & 1) ? wv : wk) + 4096 * w, sb + L_R0 + 4096 * w);
    }
    const unsigned gwo = 256 * w + 16 * g;
    u32x4 g0, g1, g2, g3;
    asm volatile("s_mov_b64 exec, %6\n\tglobal_load_dwordx4 %0, %4, %5\n\tglobal_load_dwordx4 %1, %4, %5 offset:64\n\t"
                 "global_load_dwordx4 %2, %4, %5 offset:128\n\tglobal_load_dwordx4 %3, %4, %5 offset:192\n\ts_mov_b64 exec, -1"
                 : "=&v"(g0), "=&v"(g1), "=&v"(g2), "=&v"(g3) : "v"(gwo), "s"(gw), "s"(0x0001000100010001ull) : "memory");
    asm volatile("global_load_dwordx2 %0, %1, %2 sc1" : "=v"(ep) : "v"(0u), "s"(ws + WS_EPOCH) : "memory");
    const int tp = (t & 3) | ((t & 4) << 1) | ((t & 8) >> 1);
    unsigned ko = (tk >> 5) * 2048 + (g >> 1) * 512 + ((hg >> 1) & 1) * 256 + (g & 1) * 8 + ((hg & 1) ? 1024 + tp * 16 : t * 16);
    asm volatile("s_waitcnt vmcnt(0)" : "+v"(ep), "+v"(kx0), "+v"(kx1), "+v"(kx2), "+v"(kx3), "+v"(g0), "+v"(g1), "+v"(g2), "+v"(g3), "+v"(ko) :: "memory");

    f32x4 d = {0, 0, 0, 0}, dg = {0, 0, 0, 0};
    {
      lds_cptr wp = S + L_R0 + 4096 * w + l * 8;
      const bf16x8 xa[4] = {__builtin_bit_cast(bf16x8, kx0), __builtin_bit_cast(bf16x8, kx1), __builtin_bit_cast(bf16x8, kx2), __builtin_bit_cast(bf16x8, kx3)};
      const bf16x8 ga[4] = {__builtin_bit_cast(bf16x8, g0), __builtin_bit_cast(bf16x8, g1), __builtin_bit_cast(bf16x8, g2), __builtin_bit_cast(bf16x8, g3)};
      bf16x8 wf[4];
#pragma unroll
      for (int i = 0; i < 4; ++i) wf[i] = tr_frag(wp + 1024 * i, 512);
      if (hg & 1) {
#pragma unroll
        for (int i = 0; i < 4; ++i) { dg = MFMA16(ga[i], xa[i], dg); d = MFMA16(xa[i], wf[i], d); }
      } else {
#pragma unroll
        for (int i = 0; i < 4; ++i) d = MFMA16(wf[i], xa[i], d);
      }
    }
    *(LDS_AS f32x4*)(SW + L_R0 + 4096 * w + l * 16) = d;
    *(LDS_AS float*)(SW + L_R0 + 4096 * w + 1024 + l * 4) = dg[0];
    tag = MK_TAG(ep);

    LBAR();

    if (w == 0) {
#pragma unroll
      for (int i = 1; i < 4; ++i) d += *(const LDS_AS f32x4*)(S + L_R0 + 4096 * i + l * 16);
      const u32x2 kv = __builtin_bit_cast(u32x2, pack4(d));
      gst16_sc1(2 * ko, ws + WS_KG, u32x4{kv[0], (unsigned)tag, kv[1], (unsigned)tag});
      gst8_sc1(ko, ws + WS_KV, kv);
    } else if (w == 1 && (hg & 1)) {
      float dz = 0;
#pragma unroll
      for (int i = 0; i < 4; ++i) dz += *(const LDS_AS float*)(S + L_R0 + 4096 * i + 1024 + l * 4);
      if (l < 16) gst4_sc1(WS_G + (tk + l) * 4, ws, __float_as_uint(__builtin_amdgcn_rcpf(1.f + __expf(-dz))));
    }

    const unsigned fo = WS_FKV + (2 * w + ((l >> 2) & 1)) * 256 + p4 * 8, vo = WS_KV + w * 4096 + l * 16;
    unsigned o0 = (l >> 3) * 1024 + WVO;
    int ct = 16 * (xcd >> 1), slot = L_R0 - 16384;
    {
      const unsigned go = w * 8192 + l * 16;
      const char* gp = ws + WS_KG + ct * 4096;
      unsigned la = sb + L_R0 + w * 4096 + l * 8, lb = la + 1024, lc = la + 2048, ld = la + 3072;
      asm volatile("" : "+v"(la), "+v"(lb), "+v"(lc), "+v"(ld));
      LBAR();

      BAR();

      u32x4 a[8];
#pragma unroll 1
      for (int it = 0; it < (1 << 18); ++it) {
        a[0] = gld16_sc1<0>(go, gp); a[1] = gld16_sc1<1024>(go, gp); a[2] = gld16_sc1<2048>(go, gp); a[3] = gld16_sc1<3072>(go, gp);
        a[4] = gld16_sc1<0>(go, gp + 4096); a[5] = gld16_sc1<1024>(go, gp + 4096); a[6] = gld16_sc1<2048>(go, gp + 4096); a[7] = gld16_sc1<3072>(go, gp + 4096);
        if (it == 0) BAR();
        asm volatile("s_waitcnt vmcnt(0)" : "+v"(a[0]), "+v"(a[1]), "+v"(a[2]), "+v"(a[3]), "+v"(a[4]), "+v"(a[5]), "+v"(a[6]), "+v"(a[7]) :: "memory");
        unsigned bad = 0;
#pragma unroll
        for (int i = 0; i < 8; ++i) bad |= __builtin_amdgcn_bitop3_b32(a[i][1], a[i][3], (unsigned)tag, 0x7E);
        if (__builtin_amdgcn_ballot_w64(bad != 0) == 0) break;
      }

#define W2(A, I, O) asm volatile("ds_write2_b32 %0, %1, %2 offset0:" STR(O) " offset1:" STR(O) "+1" :: "v"(A), "v"(a[I][0]), "v"(a[I][2]) : "memory")
      W2(la, 0, 0); W2(la, 1, 128); W2(lb, 2, 0); W2(lb, 3, 128); W2(lc, 4, 0); W2(lc, 5, 128); W2(ld, 6, 0); W2(ld, 7, 128);
      if (w == 0) put_flag(ws + WS_FKV + (tk >> 5) * 256 + (hg & 3) * 8, tag);
      ct = (ct + 8) & 63;
      LBAR();

    }
#pragma unroll 1
    for (int c = 1; c < 8; ++c) {
      poll_flags(fo, ws + ct * 256, tag);

      const char* kp = ws + ct * 2048;
      u32x4 a0 = gld16_sc1<0>(vo, kp), a1 = gld16_sc1<1024>(vo, kp), a2 = gld16_sc1<2048>(vo, kp), a3 = gld16_sc1<3072>(vo, kp);
      asm volatile("s_waitcnt vmcnt(0)" : "+v"(a0), "+v"(a1), "+v"(a2), "+v"(a3) :: "memory");
      lds_ptr dp = SW + slot + w * 4096 + l * 16;
      *(LDS_AS u32x4*)dp = a0; *(LDS_AS u32x4*)(dp + 1024) = a1; *(LDS_AS u32x4*)(dp + 2048) = a2; *(LDS_AS u32x4*)(dp + 3072) = a3;
      if (c >= 6) {
        const char* p = (const char*)woa + 128 * cg + (4 * (c & 1) + w) * 65536;
        const unsigned dst = sb + (4 * (c & 1) + w) * 8192;
        dma4(o0, (o0 ^ 64) + 8192, o0 + 16384, (o0 ^ 64) + 24576, p, dst);
        dma4(o0, (o0 ^ 64) + 8192, o0 + 16384, (o0 ^ 64) + 24576, p + 32768, dst + 4096);
      }
      ct = (ct + 8) & 63; slot = slot == L_S2 ? L_R0 : slot - 16384;

      LBAR();

    }

    __builtin_amdgcn_s_setprio(0);
    if (w == 0) put_flag(ws + WS_FA + (tb * 8 + hg) * 256, tag);
    if (b == 0 && w == 0) put_flag(ws + WS_EPOCH, tag);
  } else {
    LANE_VARS;
    const int hd = w & 3;
    {
      const int r = 16 * hd + (l >> 3);
      const unsigned xv = (T0 + 2 * (r & 31) + (r >> 5)) * 1024 + (((l & 7) ^ ((r >> 1) & 7)) * 16), xv1 = (xv ^ 64) + 16384;
      const unsigned wv_ = r * 1024 + WVO, wv1 = (wv_ ^ 64) + 8192;
#pragma unroll
      for (int i = 0; i < 8; ++i) {
        const int kc = (rot + 4 + (i >> 1)) & 7, lo = (4 + (i >> 1)) * 8192 + hd * 2048 + (i & 1) * 1024;
        if (i == NA0) { BAR(); }
        if (i == NA1) { BAR(); }
        dma2((i & 1) ? xv1 : xv, (const char*)x + kc * 128, sb + lo, (i & 1) ? wv1 : wv_, (const char*)wq + 128 * hg + kc * 65536, sb + L_WQ + lo);
      }
    }
    unsigned wvo = (8 * ((l >> 3) & 3) + 4 * (l >> 5) + ((l >> 1) & 3)) * 3072 + (l & 1) * 16;
    const char* wsrc = (const char*)wqkv + (4 * hg + hd) * 32;
    const int ring = L_WQ + hd * 6144;
    const int th = T0 - 16 + 2 * (t & 7);
    unsigned hvo = ((th < 0 ? 0 : th) + (t >> 3)) * 1024 + g * 16;
    int ks = 2 * rot;
    bf16x8 hx[2] = {};
    auto stage_w = [&](int sl) {
      dma3(wvo, wsrc + ks * 98304, sb + ring + sl * 3072);
      asm volatile("global_load_dwordx4 %0, %1, %2" : "+v"(hx[sl]) : "v"(hvo), "s"((const char*)x + ks * 64) : "memory");
      ks = (ks + 1) & 15;
    };
    int xa = t * 128 + ((g ^ ((t >> 1) & 7)) * 16);
    f32x4 aa[4] = {}, ak[5] = {}, av[5] = {};
    f32x4 mo, mp, mh[2];
#pragma unroll
    for (int r = 0; r < 4; ++r) {
      mo[r] = ((unsigned)(t - 4 * g - r) <= 7u) ? 0.f : -__builtin_inff();
      mp[r] = (4 * g + r >= t + 9) ? 0.f : -__builtin_inff();
      mh[1][r] = tb > 0 ? mp[r] : -__builtin_inff();
      mh[0][r] = (tb > 0 && g < 2 && 4 * g + r > t) ? 0.f : -__builtin_inff();
    }
    int wr = ring + l * 8;
    asm volatile("" : "+v"(aa[0]), "+v"(aa[1]), "+v"(aa[2]), "+v"(aa[3]), "+v"(ak[0]), "+v"(ak[1]), "+v"(ak[2]), "+v"(ak[3]), "+v"(ak[4]),
                      "+v"(av[0]), "+v"(av[1]), "+v"(av[2]), "+v"(av[3]), "+v"(av[4]), "+v"(mo), "+v"(mp), "+v"(mh[0]), "+v"(mh[1]),
                      "+v"(hx[0]), "+v"(hx[1]), "+v"(xa), "+v"(wr), "+v"(wvo), "+v"(hvo));

    WAIT_BAR(8);
    WAIT_BAR(0);

    BAR();
    stage_w(0); stage_w(1);
#pragma unroll 1
    for (int j = 0; j < 8; ++j) {
#pragma unroll
      for (int h = 0; h < 2; ++h) {
        bf16x8 wf[3], xf[4];
#pragma unroll
        for (int i = 0; i < 4; ++i) xf[i] = lds_b128(S + (xa ^ (h * 64)) + i * 2048);
        asm volatile("s_waitcnt vmcnt(4)" : "+v"(hx[h]) :: "memory");
#pragma unroll
        for (int m = 0; m < 3; ++m) wf[m] = tr_frag(S + wr + h * 3072 + m * 1024, 512);
        asm volatile("s_waitcnt lgkmcnt(0)" : "+v"(wf[0]), "+v"(wf[1]), "+v"(wf[2]) :: "memory");
        ak[4] = MFMA16(wf[1], hx[h], ak[4]); av[4] = MFMA16(hx[h], wf[2], av[4]);
        asm volatile("" : "+v"(ak[4]), "+v"(av[4]));
        stage_w(h);
#pragma unroll
        for (int i = 0; i < 4; ++i) {
          aa[i] = MFMA16(wf[0], xf[i], aa[i]);
          ak[i] = MFMA16(wf[1], xf[i], ak[i]); av[i] = MFMA16(xf[i], wf[2], av[i]);
        }
      }
      xa += 8192;

      if (j == 7) asm volatile("s_waitcnt vmcnt(0)" : "+v"(hx[0]), "+v"(hx[1]) :: "memory");
      if ((ABAR >> j) & 1) { BAR(); }
    }

    const s16x4 kh_ = pack4(ak[4]), vh_ = pack4(av[4]);
#pragma unroll
    for (int tt = 0; tt < 2; ++tt) {
      s16x4 kk[3] = {kh_, pack4(ak[2 * tt]), pack4(ak[2 * tt + 1])}, vv[3] = {vh_, pack4(av[2 * tt]), pack4(av[2 * tt + 1])};
      const s16x4 on = {0x3f80, 0x3f80, 0x3f80, 0x3f80};
      s16x4 ob[2], po[2], pp[2];
      f32x4 so[2], sp[2], o[2], ls[2];
      float m[2];
      const f32x4 z = {0, 0, 0, 0};
#pragma unroll
      for (int i = 0; i < 2; ++i) {
        s16x4 q = pack4(aa[2 * tt + i] * QSCALE);
        so[i] = MFMA16K(kk[i + 1], q, mo); sp[i] = MFMA16K(kk[i], q, i ? mp : mh[tt]);
      }
#pragma unroll
      for (int i = 0; i < 2; ++i) m[i] = fmx(fmx(fmx(fmx(so[i][0], so[i][1]), so[i][2]), fmx(fmx(so[i][3], sp[i][0]), sp[i][1])), fmx(sp[i][2], sp[i][3]));
      {
        auto r = __builtin_amdgcn_permlane16_swap(__float_as_uint(m[0]), __float_as_uint(m[1]), false, false);
        float y = xmax32(fmx(__uint_as_float(r[0]), __uint_as_float(r[1])));
        auto u = __builtin_amdgcn_permlane16_swap(__float_as_uint(y), __float_as_uint(y), false, false);
        m[0] = __uint_as_float(u[0]); m[1] = __uint_as_float(u[1]);
      }
#pragma unroll
      for (int i = 0; i < 2; ++i) {
#pragma unroll
        for (int r = 0; r < 4; ++r) { so[i][r] = __builtin_amdgcn_exp2f(so[i][r] - m[i]); sp[i][r] = __builtin_amdgcn_exp2f(sp[i][r] - m[i]); }
        po[i] = pack4(so[i]); pp[i] = pack4(sp[i]);
        ls[i] = MFMA16K(on, pp[i], MFMA16K(on, po[i], z));
        o[i] = MFMA16K(vv[i], pp[i], MFMA16K(vv[i + 1], po[i], z));
      }
#pragma unroll
      for (int i = 0; i < 2; ++i) ob[i] = pack4(o[i] * __builtin_amdgcn_rcpf(ls[i][0]));
      gst16_sc1(WS_O + ((2 * tb + tt) * 64 + 4 * hg + hd) * 1024 + l * 16, ws, join16(ob[0], ob[1]));

      if (tt == 0) { BAR(); }
    }
    unsigned o0 = (l >> 3) * 1024 + WVO, o1 = (o0 ^ 64) + 8192, o2 = o0 + 16384, o3 = o1 + 16384;
    asm volatile("s_waitcnt vmcnt(0)\n\ts_barrier" : "+v"(o0), "+v"(o1), "+v"(o2), "+v"(o3) :: "memory");

    const char* p = (const char*)wob + 128 * cg + hd * 32768;
#pragma unroll
    for (int i = 0; i < 4; ++i) {
      dma4(o0, o1, o2, o3, p + i * 131072, sb + 65536 + i * 16384 + hd * 4096);
    }

  }

  LANE_VARS;
  const int to = (w >> 1) & 1, hf = w & 1, kh = w >> 2;
  const unsigned ovo = WS_O + ((2 * tbo + to) * 64 + 16 * kh + (g >> 1)) * 1024 + ((g & 1) * 32 + 16 * hf + t) * 16;
  const int tok = 64 * tbo + 2 * (16 * hf + t) + to;
  const unsigned oso = tok * 1024 + 128 * cg + 64 * kh + 32 * (g & 1) + 16 * (g >> 1);
  int wo = (8 * g + q4) * 128 + fsw * 32 + p4 * 8 + kh * 32768;
  const int rb = L_R0 + w * 2048 + l * 16;
  f32x4 r0 = {}, r1 = {};
  const char* fl = ws + WS_FA;
  const char* ob = ws;
#pragma unroll 1
  for (int pass = 0;; ++pass) {
    asm volatile("" : "+s"(pass));
    if (w == 0) poll_flags(tbo * 2048 + ((rb << 4) & 0x700), fl, tag);
    WAIT_BAR(0);

    u32x4 of[8];
    float gs;
    of[0] = gld16_sc1<0>(ovo, ob); of[1] = gld16_sc1<2048>(ovo, ob); of[2] = gld16_sc1<0>(ovo, ob + 4096); of[3] = gld16_sc1<2048>(ovo, ob + 4096);
    of[4] = gld16_sc1<0>(ovo, ob + 8192); of[5] = gld16_sc1<2048>(ovo, ob + 8192); of[6] = gld16_sc1<0>(ovo, ob + 12288); of[7] = gld16_sc1<2048>(ovo, ob + 12288);
    asm volatile("global_load_dword %0, %1, %2 sc1" : "=v"(gs) : "v"(WS_G + tok * 4), "s"(ws) : "memory");
    bf16x8 wf[4][4];
    f32x4 acc[4] = {};
#pragma unroll
    for (int j = 0; j < 4; ++j)
#pragma unroll
      for (int c = 0; c < 4; ++c) wf[j][c] = tr_frag(S + (wo ^ (32 * c)) + j * 4096, 512);
    asm volatile("s_waitcnt vmcnt(5)" : "+v"(of[0]), "+v"(of[1]), "+v"(of[2]), "+v"(of[3]) :: "memory");

#pragma unroll
    for (int j = 0; j < 4; ++j)
#pragma unroll
      for (int c = 0; c < 4; ++c) {
        acc[c] = MFMA16(wf[j][c], __builtin_bit_cast(bf16x8, of[j]), acc[c]);
        wf[j][c] = tr_frag(S + (wo ^ (32 * c)) + (4 + j) * 4096, 512);
      }
    asm volatile("s_waitcnt vmcnt(0)" : "+v"(of[4]), "+v"(of[5]), "+v"(of[6]), "+v"(of[7]), "+v"(gs) :: "memory");
#pragma unroll
    for (int j = 0; j < 4; ++j)
#pragma unroll
      for (int c = 0; c < 4; ++c) acc[c] = MFMA16(wf[j][c], __builtin_bit_cast(bf16x8, of[4 + j]), acc[c]);

    if (pass) gs = 1.f - gs;
    *(LDS_AS f32x4*)(SW + rb) = kh ? acc[0] : acc[2];
    *(LDS_AS f32x4*)(SW + rb + 1024) = kh ? acc[1] : acc[3];
    LBAR();
    lds_cptr pr = S + (rb ^ 8192);
    r0 += ((kh ? acc[2] : acc[0]) + *(const LDS_AS f32x4*)(pr)) * gs;
    r1 += ((kh ? acc[3] : acc[1]) + *(const LDS_AS f32x4*)(pr + 1024)) * gs;
    *(u32x4*)((char*)out + oso) = join16(pack4(r0), pack4(r1));
    if (pass) break;

    fl += WS_FB - WS_FA; ob += 32768; wo += 65536;
  }

}

extern "C" void kernel_launch(void* const* d_in, const int* in_sizes, int n_in, void* d_out, int out_size, void* d_ws,
                              size_t ws_size, hipStream_t stream) {
  k_fused<<<256, 1024, 0, stream>>>((const u16*)d_in[0], (const u16*)d_in[1], (const u16*)d_in[2], (const u16*)d_in[4],
                                    (const u16*)d_in[5], (const u16*)d_in[6], (const u16*)d_in[7], (const u16*)d_in[8],
                                    (char*)d_ws, (u16*)d_out);
}
```

```cpp
#include <hip/hip_runtime.h>
#include <hip/hip_bf16.h>

typedef unsigned short u16;
typedef unsigned long long u64;
typedef __attribute__((ext_vector_type(2))) float f32x2;
typedef __attribute__((ext_vector_type(2))) __bf16 bf16x2;
typedef __attribute__((ext_vector_type(4))) float f32x4;
typedef __attribute__((ext_vector_type(8))) float f32x8;
typedef __attribute__((ext_vector_type(16))) float f32x16;
typedef __attribute__((ext_vector_type(32))) float f32x32;
typedef __attribute__((ext_vector_type(16))) __bf16 bf16x16;
typedef __attribute__((ext_vector_type(8))) __bf16 bf16x8;
typedef __attribute__((ext_vector_type(4))) __bf16 bf16x4;
typedef __attribute__((ext_vector_type(4))) short s16x4;
typedef __attribute__((ext_vector_type(8))) short s16x8;
typedef __attribute__((ext_vector_type(4))) unsigned u32x4;
typedef __attribute__((ext_vector_type(2))) unsigned u32x2;
typedef __attribute__((ext_vector_type(8))) unsigned u32x8;
typedef __attribute__((ext_vector_type(16))) unsigned u32x16;
#define LDS_AS __attribute__((address_space(3)))
typedef LDS_AS const char* lds_cptr;
typedef LDS_AS char* lds_ptr;

#define MFMA32(a, b, c) __builtin_amdgcn_mfma_f32_32x32x16_bf16(a, b, c, 0, 0, 0)
#define MFMA16(a, b, c) __builtin_amdgcn_mfma_f32_16x16x32_bf16(a, b, c, 0, 0, 0)
#define MFMA16K(a, b, c) __builtin_amdgcn_mfma_f32_16x16x16bf16_1k(a, b, c, 0, 0, 0)
#define WAIT_BAR(N) asm volatile("s_waitcnt vmcnt(" #N ") lgkmcnt(0)\n\ts_barrier" ::: "memory")
#define LBAR() asm volatile("s_waitcnt lgkmcnt(0)\n\ts_barrier" ::: "memory")
#define BAR() asm volatile("s_barrier" ::: "memory")
#define STR2(x) #x
#define STR(x) STR2(x)

constexpr unsigned WS_EPOCH = 0;
constexpr unsigned WS_FKV = 4096;
constexpr unsigned WS_G = 32768;
constexpr unsigned WS_KV = 65536;
constexpr unsigned WS_FA = 262144;
constexpr unsigned WS_FB = 327680;
constexpr unsigned WS_KG = 524288;
constexpr unsigned WS_O = 1u << 20;

#define DIAG 0
#define DGB 100
#define RT() ((unsigned)__builtin_amdgcn_s_memrealtime())
#define CY() ((unsigned)__builtin_amdgcn_s_memtime())
constexpr float QSCALE = 0.25f * 1.4426950408889634f;

__device__ __forceinline__ void dma4(unsigned v0, unsigned v1, unsigned v2, unsigned v3, const void* sbase, unsigned lds) {
  unsigned sv;
  asm volatile("s_mov_b32 %0, m0\n\ts_mov_b32 m0, %6\n\ts_nop 0\n\tglobal_load_lds_dwordx4 %1, %5\n\t"
               "s_add_u32 m0, m0, 0x400\n\ts_nop 0\n\tglobal_load_lds_dwordx4 %2, %5\n\t"
               "s_add_u32 m0, m0, 0x400\n\ts_nop 0\n\tglobal_load_lds_dwordx4 %3, %5\n\t"
               "s_add_u32 m0, m0, 0x400\n\ts_nop 0\n\tglobal_load_lds_dwordx4 %4, %5\n\ts_mov_b32 m0, %0"
               : "=&s"(sv) : "v"(v0), "v"(v1), "v"(v2), "v"(v3), "s"(sbase), "s"(lds) : "memory", "scc");
}
__device__ __forceinline__ void dma2(unsigned v0, const void* s0, unsigned l0, unsigned v1, const void* s1, unsigned l1) {
  unsigned sv;
  asm volatile("s_mov_b32 %0, m0\n\ts_mov_b32 m0, %3\n\ts_nop 0\n\tglobal_load_lds_dwordx4 %1, %2\n\t"
               "s_mov_b32 m0, %6\n\ts_nop 0\n\tglobal_load_lds_dwordx4 %4, %5\n\ts_mov_b32 m0, %0"
               : "=&s"(sv) : "v"(v0), "s"(s0), "s"(l0), "v"(v1), "s"(s1), "s"(l1) : "memory");
}
__device__ __forceinline__ void dma3(unsigned v0, const void* s0, unsigned l0) {
  unsigned sv;
  asm volatile("s_mov_b32 %0, m0\n\ts_mov_b32 m0, %3\n\ts_nop 0\n\tglobal_load_lds_dwordx4 %1, %2\n\t"
               "global_load_lds_dwordx4 %1, %2 offset:1024\n\tglobal_load_lds_dwordx4 %1, %2 offset:2048\n\ts_mov_b32 m0, %0"
               : "=&s"(sv) : "v"(v0), "s"(s0), "s"(l0) : "memory");
}
template <int OFF> __device__ __forceinline__ u32x4 gld16(unsigned voff, const void* sbase) {
  u32x4 r;
  asm volatile("global_load_dwordx4 %0, %1, %2 offset:%3" : "=v"(r) : "v"(voff), "s"(sbase), "n"(OFF) : "memory");
  return r;
}
template <int OFF> __device__ __forceinline__ u32x4 gld16_sc1(unsigned voff, const void* sbase) {
  u32x4 r;
  asm volatile("global_load_dwordx4 %0, %1, %2 offset:%3 sc1" : "=v"(r) : "v"(voff), "s"(sbase), "n"(OFF) : "memory");
  return r;
}
__device__ __forceinline__ void gst16_sc1(unsigned voff, void* sbase, u32x4 v) {
  asm volatile("global_store_dwordx4 %0, %1, %2 sc1\n\ts_nop 1" :: "v"(voff), "v"(v), "s"(sbase) : "memory");
}
__device__ __forceinline__ void gst8_sc1(unsigned voff, void* sbase, u32x2 v) {
  asm volatile("global_store_dwordx2 %0, %1, %2 sc1\n\ts_nop 1" :: "v"(voff), "v"(v), "s"(sbase) : "memory");
}
__device__ __forceinline__ void gst4_sc1(unsigned voff, void* sbase, unsigned v) {
  asm volatile("global_store_dword %0, %1, %2 sc1\n\ts_nop 1" :: "v"(voff), "v"(v), "s"(sbase) : "memory");
}
__device__ __forceinline__ void put_flag(const void* p, u64 tag) {
  unsigned a; u64 v;
  asm volatile("s_mov_b64 exec, 1\n\tv_mov_b32 %0, 0\n\tv_mov_b64 %1, %2\n\tglobal_store_dwordx2 %0, %1, %3 sc1\n\ts_mov_b64 exec, -1\n\ts_nop 1"
               : "=&v"(a), "=&v"(v) : "s"(tag), "s"(p) : "memory");
}
__device__ __forceinline__ void poll_flags(unsigned voff, const void* sbase, u64 tag) {
#pragma unroll 1
  for (int it = 0; it < (1 << 20); ++it) {
    u64 f;
    asm volatile("global_load_dwordx2 %0, %1, %2 sc1\n\ts_waitcnt vmcnt(0)" : "=&v"(f) : "v"(voff), "s"(sbase) : "memory");
    if (__builtin_amdgcn_ballot_w64(f != tag) == 0) break;
    __builtin_amdgcn_s_sleep(2);
  }
}
__device__ __forceinline__ bf16x8 lds_b128(lds_cptr p) { return *(const LDS_AS bf16x8*)p; }
__device__ __forceinline__ s16x4 lds_tr(lds_cptr p) {
  return __builtin_amdgcn_ds_read_tr16_b64_v4i16((LDS_AS s16x4*)p);
}
__device__ __forceinline__ bf16x8 tr_frag(lds_cptr p, int off2) {
  s16x4 a = lds_tr(p), b = lds_tr(p + off2);
  s16x8 r = __builtin_shufflevector(a, b, 0, 1, 2, 3, 4, 5, 6, 7);
  return __builtin_bit_cast(bf16x8, r);
}
__device__ __forceinline__ unsigned pack2(float a, float b) {
  return __builtin_bit_cast(unsigned, __builtin_convertvector(f32x2{a, b}, bf16x2));
}
__device__ __forceinline__ s16x4 pack4(f32x4 a) {
  u32x2 r = {pack2(a[0], a[1]), pack2(a[2], a[3])};
  asm("" : "+v"(r));
  return __builtin_bit_cast(s16x4, r);
}
__device__ __forceinline__ float fmx(float a, float b) { return __builtin_elementwise_maximum(a, b); }
__device__ __forceinline__ u32x4 join16(s16x4 a, s16x4 b) {
  u32x2 x = __builtin_bit_cast(u32x2, a), y = __builtin_bit_cast(u32x2, b);
  auto r0 = __builtin_amdgcn_permlane16_swap(x[0], y[0], false, false);
  auto r1 = __builtin_amdgcn_permlane16_swap(x[1], y[1], false, false);
  return u32x4{r0[0], r1[0], r0[1], r1[1]};
}
__device__ __forceinline__ float xmax32(float v) {
  auto r = __builtin_amdgcn_permlane32_swap(__float_as_uint(v), __float_as_uint(v), false, false);
  return fmx(__uint_as_float(r[0]), __uint_as_float(r[1]));
}
__device__ __forceinline__ float xmax16(float v) {
  auto r = __builtin_amdgcn_permlane16_swap(__float_as_uint(v), __float_as_uint(v), false, false);
  return fmx(__uint_as_float(r[0]), __uint_as_float(r[1]));
}
__device__ __forceinline__ float max16(const f32x16& s) {
  float a = fmx(fmx(s[0], s[1]), s[2]);
#pragma unroll
  for (int i = 3; i < 15; i += 2) a = fmx(fmx(a, s[i]), s[i + 1]);
  return fmx(a, s[15]);
}

constexpr int L_WQ = 65536, L_R0 = 147456;
constexpr int L_S2 = 114688;

#define EX(r) "v_exp_f32 v" STR(r) ", v" STR(r) "\n\t"
#define E3(a, b, c) EX(a) EX(b) EX(c)
#define E5(a, b, c, d, e) EX(a) EX(b) EX(c) EX(d) EX(e)
#define SM(V, S) "v_smfmac_f32_32x32x32_bf16 v[112:127], " V ", " S ", v60\n\t"
#define MF(S, KA, KOFF) "s_waitcnt lgkmcnt(0)\n\tv_mfma_f32_32x32x16_bf16 " S ", v[44:47], v[40:43], v[64:79]\n\tds_read_b128 v[44:47], " KA " offset:" STR(KOFF) "\n\t"
#define VX(V1, VA, O0, O1) "s_mov_b64 exec, %[mask]\n\tds_read_b128 v[48:51], " VA " offset:" STR(O0) "\n\tds_read_b128 " V1 ", " VA " offset:" STR(O1) "\n\ts_mov_b64 exec, -1\n\t"
#define SP(p) "s_setprio " STR(p) "\n\t"
#define T32E(P, KA, KOFF, VA, O0, O1) E3(80, 81, 82) SP(P) SM("v[56:59]", "v[104:111]") E5(83, 84, 85, 86, 87) MF("v[96:111]", KA, KOFF) \
  E5(88, 89, 90, 91, 92) SM("v[48:51]", "v[80:87]") VX("v[56:59]", VA, O0, O1) E3(93, 94, 95)
#define T32O(P, KA, KOFF, VA, O0, O1) E3(96, 97, 98) SP(P) SM("v[52:55]", "v[88:95]") E5(99, 100, 101, 102, 103) MF("v[80:95]", KA, KOFF) \
  E5(104, 105, 106, 107, 108) SM("v[48:51]", "v[96:103]") VX("v[52:55]", VA, O0, O1) E3(109, 110, 111)
constexpr u64 VMASK = 0x0000FFFF0000FFFFull;
#define P0 3
#define P1 2
#define P2 1
#define P3 0

#define CL4(a, b, c, d) "v" #a, "v" #b, "v" #c, "v" #d
#define CL10(a) "v" STR(a##0), "v" STR(a##1), "v" STR(a##2), "v" STR(a##3), "v" STR(a##4), "v" STR(a##5), "v" STR(a##6), "v" STR(a##7), "v" STR(a##8), "v" STR(a##9)
__device__ __forceinline__ void att_loop(u32x4 q, unsigned ones, unsigned lane, f32x16& oo, unsigned& c0) {
  unsigned cnt = 8, nx = L_R0 - 16384;
  asm volatile(
      "v_mov_b32 v48, %[ones]\n\tv_mov_b32 v49, %[ones]\n\tv_mov_b32 v60, 0xdddd\n\t"
      "v_add_u32 v61, 0x24000, v63\n\tv_add_u32 v62, 0x20000, v63\n\t"
      "v_mov_b64 v[50:51], v[48:49]\n\tv_mov_b64 v[52:53], v[48:49]\n\tv_mov_b64 v[54:55], v[48:49]\n\tv_mov_b64 v[56:57], v[48:49]\n\tv_mov_b64 v[58:59], v[48:49]\n\t"
      ".set zr%=, 104\n\t.rept 12\n\tv_mov_b64 v[zr%=:zr%=+1], 0\n\t.set zr%=, zr%=+2\n\t.endr\n\t"
      "s_barrier\n\t"
      "ds_read_b128 v[96:99], v61\n\t"
      "ds_read_b128 v[44:47], v61 offset:2048\n\t"
      VX("v[52:55]", "v61", 1024, 1280)
      "s_waitcnt lgkmcnt(3)\n\t"
      "v_mfma_f32_32x32x16_bf16 v[80:95], v[96:99], v[40:43], 0\n\t"
      "s_nop 15\n\t"
      "v_max3_f32 v64, v80, v81, v82\n\tv_max3_f32 v65, v83, v84, v85\n\tv_max3_f32 v66, v86, v87, v88\n\t"
      "v_max3_f32 v67, v89, v90, v91\n\tv_max3_f32 v68, v92, v93, v94\n\tv_max3_f32 v64, v64, v65, v95\n\t"
      "v_max3_f32 v66, v66, v67, v68\n\tv_max_f32 v64, v64, v66\n\tv_mov_b32 v65, v64\n\t"
      "s_nop 1\n\t"
      "v_permlane32_swap_b32 v64, v65\n\t"
      "s_nop 1\n\t"
      "v_min_f32_e64 v64, -v64, -v65\n\t"
      "v_mov_b32 v65, v64\n\t"
      "v_mov_b64 v[66:67], v[64:65]\n\tv_mov_b64 v[68:69], v[64:65]\n\tv_mov_b64 v[70:71], v[64:65]\n\tv_mov_b64 v[72:73], v[64:65]\n\t"
      "v_mov_b64 v[74:75], v[64:65]\n\tv_mov_b64 v[76:77], v[64:65]\n\tv_mov_b64 v[78:79], v[64:65]\n\t"
      "s_nop 1\n\t"
      "v_mfma_f32_32x32x16_bf16 v[80:95], v[96:99], v[40:43], v[64:79]\n\t"
      "s_nop 15\n\t"
      "LOOP%=:\n\t"
      T32E(P0, "v61", 4096, "v61", 3072, 3328) T32O(P1, "v61", 6144, "v61", 5120, 5376)
      T32E(P2, "v61", 8192, "v61", 7168, 7424) T32O(P3, "v61", 10240, "v61", 9216, 9472)
      T32E(P0, "v61", 12288, "v61", 11264, 11520)
      "ds_read_b32 %[c0], v61\n\t"
      T32O(P1, "v61", 14336, "v61", 13312, 13568)
      "s_barrier\n\t"
      T32E(P2, "v62", 0, "v61", 15360, 15616) T32O(P3, "v62", 2048, "v62", 1024, 1280)
      "v_mov_b32 v61, v62\n\t"
      "s_sub_u32 %[nx], %[nx], 0x4000\n\t"
      "s_cmp_lt_u32 %[nx], 0x1c000\n\t"
      "s_cselect_b32 %[nx], 0x24000, %[nx]\n\t"
      "s_sub_u32 %[cnt], %[cnt], 1\n\t"
      "v_add_u32 v62, %[nx], v63\n\t"
      "s_cmp_lg_u32 %[cnt], 0\n\t"
      "s_cbranch_scc1 LOOP%=\n\t"
      SM("v[56:59]", "v[104:111]")
      "s_waitcnt lgkmcnt(0)\n\t"
      "s_nop 15\n\ts_nop 7\n\t"
      : [c0] "=&v"(c0), "=&{v[112:127]}"(oo), [cnt] "+s"(cnt), [nx] "+s"(nx)
      : "{v[40:43]}"(q), "{v63}"(lane), [ones] "v"(ones), [mask] "s"(VMASK)
      : "memory", "scc", CL4(44, 45, 46, 47), "v48", "v49", CL10(5), "v60", "v61", "v62", CL4(64, 65, 66, 67), "v68", "v69", CL10(7), CL10(8), CL10(9), CL10(10), "v110", "v111");
}

#define NA0 2
#define NA1 5
#define ABAR 0xD5

__global__ __launch_bounds__(1024, 4) void k_fused(const u16* __restrict__ x, const u16* __restrict__ wqkv,
                                                   const u16* __restrict__ woa, const u16* __restrict__ wq,
                                                   const u16* __restrict__ wk, const u16* __restrict__ wv,
                                                   const u16* __restrict__ wob, const u16* __restrict__ gw,
                                                   char* ws, u16* __restrict__ out) {
  __shared__ __attribute__((aligned(1024))) char smem[163840];
  const unsigned sb = (unsigned)(size_t)(LDS_AS char*)smem;
  lds_cptr S = (lds_cptr)smem;
  lds_ptr SW = (lds_ptr)smem;
  const int b = blockIdx.x, xcd = b & 7, jj = b >> 3;
  const int tb = 8 * (xcd >> 1) + (jj >> 2), hg = 4 * (xcd & 1) + (jj & 3);
  const int T0 = 64 * tb;
  const int rot = (tb + 2 * (jj & 3)) & 7;
  const int w = __builtin_amdgcn_readfirstlane(threadIdx.x >> 6);
#define LANE_VARS int l = threadIdx.x; asm volatile("" : "+v"(l)); l &= 63; \
  const int g = l >> 4, t = l & 15, q4 = t >> 2, p4 = l & 3, fsw = ((q4 >> 1) & 1) | ((g & 1) << 1)
#define WVO (((l & 7) ^ ((g & 1) << 1)) * 16)
#define MK_TAG(ep) ((((u64)(unsigned)__builtin_amdgcn_readfirstlane(ep[1]) << 32) | (unsigned)__builtin_amdgcn_readfirstlane(ep[0])) * 6364136223846793005ull + 1442695040888963407ull)

  const unsigned dg0 = RT(); unsigned dgv = 0;
  if (w >= 8) {
    LANE_VARS;
    const int hd = w & 3, tt = (w >> 2) & 1, n = l & 31, cw = w - 8;
    u32x2 ep;
    __builtin_amdgcn_s_setprio(2);
    {
      const int r = 8 * cw + (l >> 3);
      const unsigned xv = (T0 + 2 * (r & 31) + (r >> 5)) * 1024 + (((l & 7) ^ ((r >> 1) & 7)) * 16);
      const unsigned wv_ = r * 1024 + (WVO ^ ((cw & 1) * 64));
#pragma unroll
      for (int i = 0; i < 4; ++i) {
        const int kc = (rot + i) & 7;
        dma2(xv, (const char*)x + kc * 128, sb + i * 8192 + cw * 1024, wv_, (const char*)wq + 128 * hg + kc * 65536, sb + L_WQ + i * 8192 + cw * 1024);
      }
      asm volatile("global_load_dwordx2 %0, %1, %2 sc1" : "=v"(ep) : "v"(0u), "s"(ws + WS_EPOCH) : "memory");
    }

    const int wa = L_WQ + (8 * g + q4) * 128 + ((hd ^ fsw) * 32) + p4 * 8;
    const int xa = (32 * tt + t) * 128 + ((g ^ ((t >> 1) & 7)) * 16);
    f32x4 aq[2] = {};
    auto quarter = [&](int h) {
      bf16x8 wf[4], x0[4], x1[4];
#pragma unroll
      for (int i = 0; i < 4; ++i) {
        const int s = 4 * h + i;
        lds_cptr xp = S + (xa ^ ((s & 1) * 64)) + (s >> 1) * 8192;
        wf[i] = tr_frag(S + wa + s * 4096, 512); x0[i] = lds_b128(xp); x1[i] = lds_b128(xp + 2048);
      }
      __builtin_amdgcn_sched_barrier(0);
#pragma unroll
      for (int i = 0; i < 4; ++i) { aq[0] = MFMA16(wf[i], x0[i], aq[0]); aq[1] = MFMA16(wf[i], x1[i], aq[1]); }
    };
    WAIT_BAR(5);
    if (DIAG == 1) dgv = (RT() - dg0) * 20;

    quarter(0);
    asm volatile("s_waitcnt vmcnt(0)\n\ts_barrier" : "+v"(ep) :: "memory");

    quarter(1);
    BAR();

    quarter(2);
    BAR();

    quarter(3);
    const u64 tag = MK_TAG(ep);
    const u32x4 qf = join16(pack4(aq[0] * QSCALE), pack4(aq[1] * QSCALE));
    const unsigned ones = ((l & 27) == 16) ? 0x3f803f80u : 0u;
    const u32x4 one4 = {ones, ones, ones, ones};
    f32x16 oo; unsigned c0;

    if (DIAG == 5) dgv = CY();
    att_loop(qf, ones, sb + l * 16, oo, c0);
    if (DIAG == 5) dgv = (CY() - dgv) >> 3;
    if (DIAG == 3) dgv = (RT() - dg0) * 4;
    if (w == 8) { *(LDS_AS unsigned*)(SW + L_R0 - 16384 + 64) = dgv; }

    bool ok = oo[8] < 1.2676506e30f;
#pragma unroll
    for (int i = 0; i < 8; ++i) ok = ok && (fabsf(oo[i]) < 3.0e38f);
    if (__builtin_amdgcn_ballot_w64(!ok) != 0) {
      const bf16x8 qq = __builtin_bit_cast(bf16x8, qf);
      float m = -__builtin_inff();
#pragma unroll 1
      for (int kt = 0; kt < 64; ++kt) {
        bf16x8 kf;
        asm volatile("global_load_dwordx4 %0, %1, %2 sc1\n\ts_waitcnt vmcnt(0)" : "=&v"(kf) : "v"(WS_KV + kt * 2048 + l * 16), "s"(ws) : "memory");
        m = fmx(m, max16(MFMA32(kf, qq, f32x16{0})));
      }
      m = xmax32(m);
      oo = f32x16{0};
#pragma unroll 1
      for (int kt = 0; kt < 64; ++kt) {
        bf16x8 kf; u32x4 v0, v1;
        asm volatile("global_load_dwordx4 %0, %3, %4 sc1\n\tglobal_load_dwordx4 %1, %3, %4 offset:1024 sc1\n\t"
                     "global_load_dwordx4 %2, %3, %4 offset:1280 sc1\n\ts_waitcnt vmcnt(0)"
                     : "=&v"(kf), "=&v"(v0), "=&v"(v1) : "v"(WS_KV + kt * 2048 + l * 16), "s"(ws) : "memory");
        f32x16 s = MFMA32(kf, qq, f32x16{0});
        f32x8 p0, p1;
#pragma unroll
        for (int i = 0; i < 8; ++i) { p0[i] = __builtin_amdgcn_exp2f(s[i] - m); p1[i] = __builtin_amdgcn_exp2f(s[8 + i] - m); }
        if (n >= 16) { v0 = one4; v1 = one4; }
        oo = __builtin_amdgcn_smfmac_f32_32x32x32_bf16(__builtin_bit_cast(bf16x8, v0), __builtin_bit_cast(bf16x16, p0), oo, 0xdddd, 0, 0);
        oo = __builtin_amdgcn_smfmac_f32_32x32x32_bf16(__builtin_bit_cast(bf16x8, v1), __builtin_bit_cast(bf16x16, p1), oo, 0xdddd, 0, 0);
      }
    }
    {
      float f = __builtin_amdgcn_rcpf(oo[8]);
      bf16x8 ob;
#pragma unroll
      for (int j = 0; j < 8; ++j) ob[j] = (__bf16)(oo[j] * f);
      gst16_sc1(WS_O + ((2 * tb + tt) * 64 + 32 + 4 * hg + hd) * 1024 + l * 16, ws, __builtin_bit_cast(u32x4, ob));
    }
    unsigned old;
    asm volatile("s_waitcnt vmcnt(0)\n\ts_mov_b64 exec, 1\n\tds_add_rtn_u32 %0, %1, %2\n\ts_waitcnt lgkmcnt(0)\n\ts_mov_b64 exec, -1"
                 : "=&v"(old) : "v"(sb + L_R0 - 16384), "v"(1u) : "memory");
    if (__builtin_amdgcn_readfirstlane(old - c0) == 7) put_flag(ws + WS_FB + (tb * 8 + hg) * 256, tag);

    return;
  }

  u64 tag = 0;
  const int tbo = 4 * xcd + (jj >> 3), cg = jj & 7;
  if (w < 4) {
    __builtin_amdgcn_s_setprio(3);
    LANE_VARS;
    u32x2 ep;
    const int tk = T0 + 16 * (hg >> 1);
    const unsigned kxo = (unsigned)((tk + t) * 1024 + 256 * w + 16 * g);
    u32x4 kx0 = gld16<0>(kxo, x), kx1 = gld16<64>(kxo, x), kx2 = gld16<128>(kxo, x), kx3 = gld16<192>(kxo, x);
    {
      const unsigned kv = (8 * ((l >> 3) & 3) + 4 * (l >> 5) + ((l >> 1) & 3)) * 32 + (l & 1) * 16;
      dma4(kv, kv + 1024, kv + 2048, kv + 3072, (const char*)((hg & 1) ? wv : wk) + 4096 * w, sb + L_R0 + 4096 * w);
    }
    const unsigned gwo = 256 * w + 16 * g;
    u32x4 g0, g1, g2, g3;
    asm volatile("s_mov_b64 exec, %6\n\tglobal_load_dwordx4 %0, %4, %5\n\tglobal_load_dwordx4 %1, %4, %5 offset:64\n\t"
                 "global_load_dwordx4 %2, %4, %5 offset:128\n\tglobal_load_dwordx4 %3, %4, %5 offset:192\n\ts_mov_b64 exec, -1"
                 : "=&v"(g0), "=&v"(g1), "=&v"(g2), "=&v"(g3) : "v"(gwo), "s"(gw), "s"(0x0001000100010001ull) : "memory");
    asm volatile("global_load_dwordx2 %0, %1, %2 sc1" : "=v"(ep) : "v"(0u), "s"(ws + WS_EPOCH) : "memory");
    const int tp = (t & 3) | ((t & 4) << 1) | ((t & 8) >> 1);
    unsigned ko = (tk >> 5) * 2048 + (g >> 1) * 512 + ((hg >> 1) & 1) * 256 + (g & 1) * 8 + ((hg & 1) ? 1024 + tp * 16 : t * 16);
    asm volatile("s_waitcnt vmcnt(0)" : "+v"(ep), "+v"(kx0), "+v"(kx1), "+v"(kx2), "+v"(kx3), "+v"(g0), "+v"(g1), "+v"(g2), "+v"(g3), "+v"(ko) :: "memory");

    f32x4 d = {0, 0, 0, 0}, dg = {0, 0, 0, 0};
    {
      lds_cptr wp = S + L_R0 + 4096 * w + l * 8;
      const bf16x8 xa[4] = {__builtin_bit_cast(bf16x8, kx0), __builtin_bit_cast(bf16x8, kx1), __builtin_bit_cast(bf16x8, kx2), __builtin_bit_cast(bf16x8, kx3)};
      const bf16x8 ga[4] = {__builtin_bit_cast(bf16x8, g0), __builtin_bit_cast(bf16x8, g1), __builtin_bit_cast(bf16x8, g2), __builtin_bit_cast(bf16x8, g3)};
      bf16x8 wf[4];
#pragma unroll
      for (int i = 0; i < 4; ++i) wf[i] = tr_frag(wp + 1024 * i, 512);
      if (hg & 1) {
#pragma unroll
        for (int i = 0; i < 4; ++i) { dg = MFMA16(ga[i], xa[i], dg); d = MFMA16(xa[i], wf[i], d); }
      } else {
#pragma unroll
        for (int i = 0; i < 4; ++i) d = MFMA16(wf[i], xa[i], d);
      }
    }
    *(LDS_AS f32x4*)(SW + L_R0 + 4096 * w + l * 16) = d;
    *(LDS_AS float*)(SW + L_R0 + 4096 * w + 1024 + l * 4) = dg[0];
    tag = MK_TAG(ep);

    LBAR();

    if (w == 0) {
#pragma unroll
      for (int i = 1; i < 4; ++i) d += *(const LDS_AS f32x4*)(S + L_R0 + 4096 * i + l * 16);
      const u32x2 kv = __builtin_bit_cast(u32x2, pack4(d));
      gst16_sc1(2 * ko, ws + WS_KG, u32x4{kv[0], (unsigned)tag, kv[1], (unsigned)tag});
      gst8_sc1(ko, ws + WS_KV, kv);
    } else if (w == 1 && (hg & 1)) {
      float dz = 0;
#pragma unroll
      for (int i = 0; i < 4; ++i) dz += *(const LDS_AS float*)(S + L_R0 + 4096 * i + 1024 + l * 4);
      if (l < 16) gst4_sc1(WS_G + (tk + l) * 4, ws, __float_as_uint(__builtin_amdgcn_rcpf(1.f + __expf(-dz))));
    }

    const unsigned fo = WS_FKV + (2 * w + ((l >> 2) & 1)) * 256 + p4 * 8, vo = WS_KV + w * 4096 + l * 16;
    unsigned o0 = (l >> 3) * 1024 + WVO;
    int ct = 16 * (xcd >> 1), slot = L_R0 - 16384;
    {
      const unsigned go = w * 8192 + l * 16;
      const char* gp = ws + WS_KG + ct * 4096;
      unsigned la = sb + L_R0 + w * 4096 + l * 8, lb = la + 1024, lc = la + 2048, ld = la + 3072;
      asm volatile("" : "+v"(la), "+v"(lb), "+v"(lc), "+v"(ld));
      LBAR();

      BAR();

      u32x4 a[8];
#pragma unroll 1
      for (int it = 0; it < (1 << 18); ++it) {
        a[0] = gld16_sc1<0>(go, gp); a[1] = gld16_sc1<1024>(go, gp); a[2] = gld16_sc1<2048>(go, gp); a[3] = gld16_sc1<3072>(go, gp);
        a[4] = gld16_sc1<0>(go, gp + 4096); a[5] = gld16_sc1<1024>(go, gp + 4096); a[6] = gld16_sc1<2048>(go, gp + 4096); a[7] = gld16_sc1<3072>(go, gp + 4096);
        if (it == 0) BAR();
        asm volatile("s_waitcnt vmcnt(0)" : "+v"(a[0]), "+v"(a[1]), "+v"(a[2]), "+v"(a[3]), "+v"(a[4]), "+v"(a[5]), "+v"(a[6]), "+v"(a[7]) :: "memory");
        unsigned bad = 0;
#pragma unroll
        for (int i = 0; i < 8; ++i) bad |= __builtin_amdgcn_bitop3_b32(a[i][1], a[i][3], (unsigned)tag, 0x7E);
        if (__builtin_amdgcn_ballot_w64(bad != 0) == 0) break;
      }

#define W2(A, I, O) asm volatile("ds_write2_b32 %0, %1, %2 offset0:" STR(O) " offset1:" STR(O) "+1" :: "v"(A), "v"(a[I][0]), "v"(a[I][2]) : "memory")
      W2(la, 0, 0); W2(la, 1, 128); W2(lb, 2, 0); W2(lb, 3, 128); W2(lc, 4, 0); W2(lc, 5, 128); W2(ld, 6, 0); W2(ld, 7, 128);
      if (w == 0) put_flag(ws + WS_FKV + (tk >> 5) * 256 + (hg & 3) * 8, tag);
      ct = (ct + 8) & 63;
      LBAR();
      if (DIAG == 2) dgv = (RT() - dg0) * 10;

    }
#pragma unroll 1
    for (int c = 1; c < 8; ++c) {
      poll_flags(fo, ws + ct * 256, tag);

      const char* kp = ws + ct * 2048;
      u32x4 a0 = gld16_sc1<0>(vo, kp), a1 = gld16_sc1<1024>(vo, kp), a2 = gld16_sc1<2048>(vo, kp), a3 = gld16_sc1<3072>(vo, kp);
      asm volatile("s_waitcnt vmcnt(0)" : "+v"(a0), "+v"(a1), "+v"(a2), "+v"(a3) :: "memory");
      lds_ptr dp = SW + slot + w * 4096 + l * 16;
      *(LDS_AS u32x4*)dp = a0; *(LDS_AS u32x4*)(dp + 1024) = a1; *(LDS_AS u32x4*)(dp + 2048) = a2; *(LDS_AS u32x4*)(dp + 3072) = a3;
      if (c >= 6) {
        const char* p = (const char*)woa + 128 * cg + (4 * (c & 1) + w) * 65536;
        const unsigned dst = sb + (4 * (c & 1) + w) * 8192;
        dma4(o0, (o0 ^ 64) + 8192, o0 + 16384, (o0 ^ 64) + 24576, p, dst);
        dma4(o0, (o0 ^ 64) + 8192, o0 + 16384, (o0 ^ 64) + 24576, p + 32768, dst + 4096);
      }
      ct = (ct + 8) & 63; slot = slot == L_S2 ? L_R0 : slot - 16384;

      LBAR();

    }

    __builtin_amdgcn_s_setprio(0);
    if (w == 0) put_flag(ws + WS_FA + (tb * 8 + hg) * 256, tag);
    if (b == 0 && w == 0) put_flag(ws + WS_EPOCH, tag);
  } else {
    LANE_VARS;
    const int hd = w & 3;
    {
      const int r = 16 * hd + (l >> 3);
      const unsigned xv = (T0 + 2 * (r & 31) + (r >> 5)) * 1024 + (((l & 7) ^ ((r >> 1) & 7)) * 16), xv1 = (xv ^ 64) + 16384;
      const unsigned wv_ = r * 1024 + WVO, wv1 = (wv_ ^ 64) + 8192;
#pragma unroll
      for (int i = 0; i < 8; ++i) {
        const int kc = (rot + 4 + (i >> 1)) & 7, lo = (4 + (i >> 1)) * 8192 + hd * 2048 + (i & 1) * 1024;
        if (i == NA0) { BAR(); }
        if (i == NA1) { BAR(); }
        dma2((i & 1) ? xv1 : xv, (const char*)x + kc * 128, sb + lo, (i & 1) ? wv1 : wv_, (const char*)wq + 128 * hg + kc * 65536, sb + L_WQ + lo);
      }
    }
    unsigned wvo = (8 * ((l >> 3) & 3) + 4 * (l >> 5) + ((l >> 1) & 3)) * 3072 + (l & 1) * 16;
    const char* wsrc = (const char*)wqkv + (4 * hg + hd) * 32;
    const int ring = L_WQ + hd * 6144;
    const int th = T0 - 16 + 2 * (t & 7);
    unsigned hvo = ((th < 0 ? 0 : th) + (t >> 3)) * 1024 + g * 16;
    int ks = 2 * rot;
    bf16x8 hx[2] = {};
    auto stage_w = [&](int sl) {
      dma3(wvo, wsrc + ks * 98304, sb + ring + sl * 3072);
      asm volatile("global_load_dwordx4 %0, %1, %2" : "+v"(hx[sl]) : "v"(hvo), "s"((const char*)x + ks * 64) : "memory");
      ks = (ks + 1) & 15;
    };
    int xa = t * 128 + ((g ^ ((t >> 1) & 7)) * 16);
    f32x4 aa[4] = {}, ak[5] = {}, av[5] = {};
    f32x4 mo, mp, mh[2];
#pragma unroll
    for (int r = 0; r < 4; ++r) {
      mo[r] = ((unsigned)(t - 4 * g - r) <= 7u) ? 0.f : -__builtin_inff();
      mp[r] = (4 * g + r >= t + 9) ? 0.f : -__builtin_inff();
      mh[1][r] = tb > 0 ? mp[r] : -__builtin_inff();
      mh[0][r] = (tb > 0 && g < 2 && 4 * g + r > t) ? 0.f : -__builtin_inff();
    }
    int wr = ring + l * 8;
    asm volatile("" : "+v"(aa[0]), "+v"(aa[1]), "+v"(aa[2]), "+v"(aa[3]), "+v"(ak[0]), "+v"(ak[1]), "+v"(ak[2]), "+v"(ak[3]), "+v"(ak[4]),
                      "+v"(av[0]), "+v"(av[1]), "+v"(av[2]), "+v"(av[3]), "+v"(av[4]), "+v"(mo), "+v"(mp), "+v"(mh[0]), "+v"(mh[1]),
                      "+v"(hx[0]), "+v"(hx[1]), "+v"(xa), "+v"(wr), "+v"(wvo), "+v"(hvo));

    WAIT_BAR(8);
    WAIT_BAR(0);

    BAR();
    stage_w(0); stage_w(1);
#pragma unroll 1
    for (int j = 0; j < 8; ++j) {
#pragma unroll
      for (int h = 0; h < 2; ++h) {
        bf16x8 wf[3], xf[4];
#pragma unroll
        for (int i = 0; i < 4; ++i) xf[i] = lds_b128(S + (xa ^ (h * 64)) + i * 2048);
        asm volatile("s_waitcnt vmcnt(4)" : "+v"(hx[h]) :: "memory");
#pragma unroll
        for (int m = 0; m < 3; ++m) wf[m] = tr_frag(S + wr + h * 3072 + m * 1024, 512);
        asm volatile("s_waitcnt lgkmcnt(0)" : "+v"(wf[0]), "+v"(wf[1]), "+v"(wf[2]) :: "memory");
        ak[4] = MFMA16(wf[1], hx[h], ak[4]); av[4] = MFMA16(hx[h], wf[2], av[4]);
        asm volatile("" : "+v"(ak[4]), "+v"(av[4]));
        stage_w(h);
#pragma unroll
        for (int i = 0; i < 4; ++i) {
          aa[i] = MFMA16(wf[0], xf[i], aa[i]);
          ak[i] = MFMA16(wf[1], xf[i], ak[i]); av[i] = MFMA16(xf[i], wf[2], av[i]);
        }
      }
      xa += 8192;

      if (j == 7) asm volatile("s_waitcnt vmcnt(0)" : "+v"(hx[0]), "+v"(hx[1]) :: "memory");
      if ((ABAR >> j) & 1) { BAR(); }
    }

    const s16x4 kh_ = pack4(ak[4]), vh_ = pack4(av[4]);
#pragma unroll
    for (int tt = 0; tt < 2; ++tt) {
      s16x4 kk[3] = {kh_, pack4(ak[2 * tt]), pack4(ak[2 * tt + 1])}, vv[3] = {vh_, pack4(av[2 * tt]), pack4(av[2 * tt + 1])};
      const s16x4 on = {0x3f80, 0x3f80, 0x3f80, 0x3f80};
      s16x4 ob[2], po[2], pp[2];
      f32x4 so[2], sp[2], o[2], ls[2];
      float m[2];
      const f32x4 z = {0, 0, 0, 0};
#pragma unroll
      for (int i = 0; i < 2; ++i) {
        s16x4 q = pack4(aa[2 * tt + i] * QSCALE);
        so[i] = MFMA16K(kk[i + 1], q, mo); sp[i] = MFMA16K(kk[i], q, i ? mp : mh[tt]);
      }
#pragma unroll
      for (int i = 0; i < 2; ++i) m[i] = fmx(fmx(fmx(fmx(so[i][0], so[i][1]), so[i][2]), fmx(fmx(so[i][3], sp[i][0]), sp[i][1])), fmx(sp[i][2], sp[i][3]));
      {
        auto r = __builtin_amdgcn_permlane16_swap(__float_as_uint(m[0]), __float_as_uint(m[1]), false, false);
        float y = xmax32(fmx(__uint_as_float(r[0]), __uint_as_float(r[1])));
        auto u = __builtin_amdgcn_permlane16_swap(__float_as_uint(y), __float_as_uint(y), false, false);
        m[0] = __uint_as_float(u[0]); m[1] = __uint_as_float(u[1]);
      }
#pragma unroll
      for (int i = 0; i < 2; ++i) {
#pragma unroll
        for (int r = 0; r < 4; ++r) { so[i][r] = __builtin_amdgcn_exp2f(so[i][r] - m[i]); sp[i][r] = __builtin_amdgcn_exp2f(sp[i][r] - m[i]); }
        po[i] = pack4(so[i]); pp[i] = pack4(sp[i]);
        ls[i] = MFMA16K(on, pp[i], MFMA16K(on, po[i], z));
        o[i] = MFMA16K(vv[i], pp[i], MFMA16K(vv[i + 1], po[i], z));
      }
#pragma unroll
      for (int i = 0; i < 2; ++i) ob[i] = pack4(o[i] * __builtin_amdgcn_rcpf(ls[i][0]));
      gst16_sc1(WS_O + ((2 * tb + tt) * 64 + 4 * hg + hd) * 1024 + l * 16, ws, join16(ob[0], ob[1]));

      if (tt == 0) { BAR(); }
    }
    unsigned o0 = (l >> 3) * 1024 + WVO, o1 = (o0 ^ 64) + 8192, o2 = o0 + 16384, o3 = o1 + 16384;
    asm volatile("s_waitcnt vmcnt(0)\n\ts_barrier" : "+v"(o0), "+v"(o1), "+v"(o2), "+v"(o3) :: "memory");

    const char* p = (const char*)wob + 128 * cg + hd * 32768;
#pragma unroll
    for (int i = 0; i < 4; ++i) {
      dma4(o0, o1, o2, o3, p + i * 131072, sb + 65536 + i * 16384 + hd * 4096);
    }

  }

  LANE_VARS;
  const int to = (w >> 1) & 1, hf = w & 1, kh = w >> 2;
  const unsigned ovo = WS_O + ((2 * tbo + to) * 64 + 16 * kh + (g >> 1)) * 1024 + ((g & 1) * 32 + 16 * hf + t) * 16;
  const int tok = 64 * tbo + 2 * (16 * hf + t) + to;
  const unsigned oso = tok * 1024 + 128 * cg + 64 * kh + 32 * (g & 1) + 16 * (g >> 1);
  int wo = (8 * g + q4) * 128 + fsw * 32 + p4 * 8 + kh * 32768;
  const int rb = L_R0 + w * 2048 + l * 16;
  f32x4 r0 = {}, r1 = {};
  const char* fl = ws + WS_FA;
  const char* ob = ws;
#pragma unroll 1
  for (int pass = 0;; ++pass) {
    asm volatile("" : "+s"(pass));
    if (w == 0) poll_flags(tbo * 2048 + ((rb << 4) & 0x700), fl, tag);
    WAIT_BAR(0);

    u32x4 of[8];
    float gs;
    of[0] = gld16_sc1<0>(ovo, ob); of[1] = gld16_sc1<2048>(ovo, ob); of[2] = gld16_sc1<0>(ovo, ob + 4096); of[3] = gld16_sc1<2048>(ovo, ob + 4096);
    of[4] = gld16_sc1<0>(ovo, ob + 8192); of[5] = gld16_sc1<2048>(ovo, ob + 8192); of[6] = gld16_sc1<0>(ovo, ob + 12288); of[7] = gld16_sc1<2048>(ovo, ob + 12288);
    asm volatile("global_load_dword %0, %1, %2 sc1" : "=v"(gs) : "v"(WS_G + tok * 4), "s"(ws) : "memory");
    bf16x8 wf[4][4];
    f32x4 acc[4] = {};
#pragma unroll
    for (int j = 0; j < 4; ++j)
#pragma unroll
      for (int c = 0; c < 4; ++c) wf[j][c] = tr_frag(S + (wo ^ (32 * c)) + j * 4096, 512);
    asm volatile("s_waitcnt vmcnt(5)" : "+v"(of[0]), "+v"(of[1]), "+v"(of[2]), "+v"(of[3]) :: "memory");

#pragma unroll
    for (int j = 0; j < 4; ++j)
#pragma unroll
      for (int c = 0; c < 4; ++c) {
        acc[c] = MFMA16(wf[j][c], __builtin_bit_cast(bf16x8, of[j]), acc[c]);
        wf[j][c] = tr_frag(S + (wo ^ (32 * c)) + (4 + j) * 4096, 512);
      }
    asm volatile("s_waitcnt vmcnt(0)" : "+v"(of[4]), "+v"(of[5]), "+v"(of[6]), "+v"(of[7]), "+v"(gs) :: "memory");
#pragma unroll
    for (int j = 0; j < 4; ++j)
#pragma unroll
      for (int c = 0; c < 4; ++c) acc[c] = MFMA16(wf[j][c], __builtin_bit_cast(bf16x8, of[4 + j]), acc[c]);

    if (pass) gs = 1.f - gs;
    *(LDS_AS f32x4*)(SW + rb) = kh ? acc[0] : acc[2];
    *(LDS_AS f32x4*)(SW + rb + 1024) = kh ? acc[1] : acc[3];
    LBAR();
    lds_cptr pr = S + (rb ^ 8192);
    r0 += ((kh ? acc[2] : acc[0]) + *(const LDS_AS f32x4*)(pr)) * gs;
    r1 += ((kh ? acc[3] : acc[1]) + *(const LDS_AS f32x4*)(pr + 1024)) * gs;
    *(u32x4*)((char*)out + oso) = join16(pack4(r0), pack4(r1));
    if (pass) break;

    fl += WS_FB - WS_FA; ob += 32768; wo += 65536;
  }
  if (w == 0 && b == DGB) {
    if (DIAG == 4) dgv = (RT() - dg0) * 4;
    if (DIAG == 1 || DIAG == 3 || DIAG == 5) dgv = __builtin_amdgcn_readfirstlane(*(const LDS_AS unsigned*)(S + L_R0 - 16384 + 64));
    if (dgv > 20000) dgv = 20000;
    for (int i = 0; i < 1000000; ++i) { if (RT() - dg0 >= 4000 + dgv) break; __builtin_amdgcn_s_sleep(1); }
  }

}

extern "C" void kernel_launch(void* const* d_in, const int* in_sizes, int n_in, void* d_out, int out_size, void* d_ws,
                              size_t ws_size, hipStream_t stream) {
  k_fused<<<256, 1024, 0, stream>>>((const u16*)d_in[0], (const u16*)d_in[1], (const u16*)d_in[2], (const u16*)d_in[4],
                                    (const u16*)d_in[5], (const u16*)d_in[6], (const u16*)d_in[7], (const u16*)d_in[8],
                                    (char*)d_ws, (u16*)d_out);
}
```

```cpp
#include <hip/hip_runtime.h>
#include <hip/hip_bf16.h>

typedef unsigned short u16;
typedef unsigned long long u64;
typedef __attribute__((ext_vector_type(2))) float f32x2;
typedef __attribute__((ext_vector_type(2))) __bf16 bf16x2;
typedef __attribute__((ext_vector_type(4))) float f32x4;
typedef __attribute__((ext_vector_type(8))) float f32x8;
typedef __attribute__((ext_vector_type(16))) float f32x16;
typedef __attribute__((ext_vector_type(32))) float f32x32;
typedef __attribute__((ext_vector_type(16))) __bf16 bf16x16;
typedef __attribute__((ext_vector_type(8))) __bf16 bf16x8;
typedef __attribute__((ext_vector_type(4))) __bf16 bf16x4;
typedef __attribute__((ext_vector_type(4))) short s16x4;
typedef __attribute__((ext_vector_type(8))) short s16x8;
typedef __attribute__((ext_vector_type(4))) unsigned u32x4;
typedef __attribute__((ext_vector_type(2))) unsigned u32x2;
typedef __attribute__((ext_vector_type(8))) unsigned u32x8;
typedef __attribute__((ext_vector_type(16))) unsigned u32x16;
#define LDS_AS __attribute__((address_space(3)))
typedef LDS_AS const char* lds_cptr;
typedef LDS_AS char* lds_ptr;

#define MFMA32(a, b, c) __builtin_amdgcn_mfma_f32_32x32x16_bf16(a, b, c, 0, 0, 0)
#define MFMA16(a, b, c) __builtin_amdgcn_mfma_f32_16x16x32_bf16(a, b, c, 0, 0, 0)
#define MFMA16K(a, b, c) __builtin_amdgcn_mfma_f32_16x16x16bf16_1k(a, b, c, 0, 0, 0)
#define WAIT_BAR(N) asm volatile("s_waitcnt vmcnt(" #N ") lgkmcnt(0)\n\ts_barrier" ::: "memory")
#define LBAR() asm volatile("s_waitcnt lgkmcnt(0)\n\ts_barrier" ::: "memory")
#define BAR() asm volatile("s_barrier" ::: "memory")
#define STR2(x) #x
#define STR(x) STR2(x)

constexpr unsigned WS_EPOCH = 0;
constexpr unsigned WS_FKV = 4096;
constexpr unsigned WS_G = 32768;
constexpr unsigned WS_KV = 65536;
constexpr unsigned WS_FA = 262144;
constexpr unsigned WS_FB = 327680;
constexpr unsigned WS_KG = 524288;
constexpr unsigned WS_O = 1u << 20;

constexpr float QSCALE = 0.25f * 1.4426950408889634f;

__device__ __forceinline__ void dma4(unsigned v0, unsigned v1, unsigned v2, unsigned v3, const void* sbase, unsigned lds) {
  unsigned sv;
  asm volatile("s_mov_b32 %0, m0\n\ts_mov_b32 m0, %6\n\ts_nop 0\n\tglobal_load_lds_dwordx4 %1, %5\n\t"
               "s_add_u32 m0, m0, 0x400\n\ts_nop 0\n\tglobal_load_lds_dwordx4 %2, %5\n\t"
               "s_add_u32 m0, m0, 0x400\n\ts_nop 0\n\tglobal_load_lds_dwordx4 %3, %5\n\t"
               "s_add_u32 m0, m0, 0x400\n\ts_nop 0\n\tglobal_load_lds_dwordx4 %4, %5\n\ts_mov_b32 m0, %0"
               : "=&s"(sv) : "v"(v0), "v"(v1), "v"(v2), "v"(v3), "s"(sbase), "s"(lds) : "memory", "scc");
}
__device__ __forceinline__ void dma2(unsigned v0, const void* s0, unsigned l0, unsigned v1, const void* s1, unsigned l1) {
  unsigned sv;
  asm volatile("s_mov_b32 %0, m0\n\ts_mov_b32 m0, %3\n\ts_nop 0\n\tglobal_load_lds_dwordx4 %1, %2\n\t"
               "s_mov_b32 m0, %6\n\ts_nop 0\n\tglobal_load_lds_dwordx4 %4, %5\n\ts_mov_b32 m0, %0"
               : "=&s"(sv) : "v"(v0), "s"(s0), "s"(l0), "v"(v1), "s"(s1), "s"(l1) : "memory");
}
__device__ __forceinline__ void dma3(unsigned v0, const void* s0, unsigned l0) {
  unsigned sv;
  asm volatile("s_mov_b32 %0, m0\n\ts_mov_b32 m0, %3\n\ts_nop 0\n\tglobal_load_lds_dwordx4 %1, %2\n\t"
               "global_load_lds_dwordx4 %1, %2 offset:1024\n\tglobal_load_lds_dwordx4 %1, %2 offset:2048\n\ts_mov_b32 m0, %0"
               : "=&s"(sv) : "v"(v0), "s"(s0), "s"(l0) : "memory");
}
template <int OFF> __device__ __forceinline__ u32x4 gld16(unsigned voff, const void* sbase) {
  u32x4 r;
  asm volatile("global_load_dwordx4 %0, %1, %2 offset:%3" : "=v"(r) : "v"(voff), "s"(sbase), "n"(OFF) : "memory");
  return r;
}
template <int OFF> __device__ __forceinline__ u32x4 gld16_sc1(unsigned voff, const void* sbase) {
  u32x4 r;
  asm volatile("global_load_dwordx4 %0, %1, %2 offset:%3 sc1" : "=v"(r) : "v"(voff), "s"(sbase), "n"(OFF) : "memory");
  return r;
}
__device__ __forceinline__ void gst16_sc1(unsigned voff, void* sbase, u32x4 v) {
  asm volatile("global_store_dwordx4 %0, %1, %2 sc1\n\ts_nop 1" :: "v"(voff), "v"(v), "s"(sbase) : "memory");
}
__device__ __forceinline__ void gst8_sc1(unsigned voff, void* sbase, u32x2 v) {
  asm volatile("global_store_dwordx2 %0, %1, %2 sc1\n\ts_nop 1" :: "v"(voff), "v"(v), "s"(sbase) : "memory");
}
__device__ __forceinline__ void gst4_sc1(unsigned voff, void* sbase, unsigned v) {
  asm volatile("global_store_dword %0, %1, %2 sc1\n\ts_nop 1" :: "v"(voff), "v"(v), "s"(sbase) : "memory");
}
__device__ __forceinline__ void put_flag(const void* p, u64 tag) {
  unsigned a; u64 v;
  asm volatile("s_mov_b64 exec, 1\n\tv_mov_b32 %0, 0\n\tv_mov_b64 %1, %2\n\tglobal_store_dwordx2 %0, %1, %3 sc1\n\ts_mov_b64 exec, -1\n\ts_nop 1"
               : "=&v"(a), "=&v"(v) : "s"(tag), "s"(p) : "memory");
}
__device__ __forceinline__ unsigned arrive(unsigned lds, unsigned c0) {
  unsigned old;
  asm volatile("s_waitcnt vmcnt(0)\n\ts_mov_b64 exec, 1\n\tds_add_rtn_u32 %0, %1, %2\n\ts_waitcnt lgkmcnt(0)\n\ts_mov_b64 exec, -1"
               : "=&v"(old) : "v"(lds), "v"(1u) : "memory");
  return __builtin_amdgcn_readfirstlane(old - c0);
}
__device__ __forceinline__ void poll_flags(unsigned voff, const void* sbase, u64 tag) {
#pragma unroll 1
  for (int it = 0; it < (1 << 20); ++it) {
    u64 f;
    asm volatile("global_load_dwordx2 %0, %1, %2 sc1\n\ts_waitcnt vmcnt(0)" : "=&v"(f) : "v"(voff), "s"(sbase) : "memory");
    if (__builtin_amdgcn_ballot_w64(f != tag) == 0) break;
    __builtin_amdgcn_s_sleep(2);
  }
}
__device__ __forceinline__ bf16x8 lds_b128(lds_cptr p) { return *(const LDS_AS bf16x8*)p; }
__device__ __forceinline__ s16x4 lds_tr(lds_cptr p) {
  return __builtin_amdgcn_ds_read_tr16_b64_v4i16((LDS_AS s16x4*)p);
}
__device__ __forceinline__ bf16x8 tr_frag(lds_cptr p, int off2) {
  s16x4 a = lds_tr(p), b = lds_tr(p + off2);
  s16x8 r = __builtin_shufflevector(a, b, 0, 1, 2, 3, 4, 5, 6, 7);
  return __builtin_bit_cast(bf16x8, r);
}
__device__ __forceinline__ unsigned pack2(float a, float b) {
  return __builtin_bit_cast(unsigned, __builtin_convertvector(f32x2{a, b}, bf16x2));
}
__device__ __forceinline__ s16x4 pack4(f32x4 a) {
  u32x2 r = {pack2(a[0], a[1]), pack2(a[2], a[3])};
  asm("" : "+v"(r));
  return __builtin_bit_cast(s16x4, r);
}
__device__ __forceinline__ float fmx(float a, float b) { return __builtin_elementwise_maximum(a, b); }
__device__ __forceinline__ u32x4 join16(s16x4 a, s16x4 b) {
  u32x2 x = __builtin_bit_cast(u32x2, a), y = __builtin_bit_cast(u32x2, b);
  auto r0 = __builtin_amdgcn_permlane16_swap(x[0], y[0], false, false);
  auto r1 = __builtin_amdgcn_permlane16_swap(x[1], y[1], false, false);
  return u32x4{r0[0], r1[0], r0[1], r1[1]};
}
__device__ __forceinline__ float xmax32(float v) {
  auto r = __builtin_amdgcn_permlane32_swap(__float_as_uint(v), __float_as_uint(v), false, false);
  return fmx(__uint_as_float(r[0]), __uint_as_float(r[1]));
}
__device__ __forceinline__ float xmax16(float v) {
  auto r = __builtin_amdgcn_permlane16_swap(__float_as_uint(v), __float_as_uint(v), false, false);
  return fmx(__uint_as_float(r[0]), __uint_as_float(r[1]));
}
__device__ __forceinline__ float max16(const f32x16& s) {
  float a = fmx(fmx(s[0], s[1]), s[2]);
#pragma unroll
  for (int i = 3; i < 15; i += 2) a = fmx(fmx(a, s[i]), s[i + 1]);
  return fmx(a, s[15]);
}

constexpr int L_WQ = 65536, L_R0 = 147456;
constexpr int L_TAG = L_R0 + 2048;
constexpr int L_S2 = 114688;

#define EX(r) "v_exp_f32 v" STR(r) ", v" STR(r) "\n\t"
#define E3(a, b, c) EX(a) EX(b) EX(c)
#define E5(a, b, c, d, e) EX(a) EX(b) EX(c) EX(d) EX(e)
#define SM(V, S) "v_smfmac_f32_32x32x32_bf16 v[112:127], " V ", " S ", v60\n\t"
#define MF(S, KA, KOFF) "s_waitcnt lgkmcnt(0)\n\tv_mfma_f32_32x32x16_bf16 " S ", v[44:47], v[40:43], v[64:79]\n\tds_read_b128 v[44:47], " KA " offset:" STR(KOFF) "\n\t"
#define VX(V1, VA, O0, O1) "s_mov_b64 exec, %[mask]\n\tds_read_b128 v[48:51], " VA " offset:" STR(O0) "\n\tds_read_b128 " V1 ", " VA " offset:" STR(O1) "\n\ts_mov_b64 exec, -1\n\t"
#define SP(p) "s_setprio " STR(p) "\n\t"
#define T32E(P, KA, KOFF, VA, O0, O1) E3(80, 81, 82) SP(P) SM("v[56:59]", "v[104:111]") E5(83, 84, 85, 86, 87) MF("v[96:111]", KA, KOFF) \
  E5(88, 89, 90, 91, 92) SM("v[48:51]", "v[80:87]") VX("v[56:59]", VA, O0, O1) E3(93, 94, 95)
#define T32O(P, KA, KOFF, VA, O0, O1) E3(96, 97, 98) SP(P) SM("v[52:55]", "v[88:95]") E5(99, 100, 101, 102, 103) MF("v[80:95]", KA, KOFF) \
  E5(104, 105, 106, 107, 108) SM("v[48:51]", "v[96:103]") VX("v[52:55]", VA, O0, O1) E3(109, 110, 111)
constexpr u64 VMASK = 0x0000FFFF0000FFFFull;
#define P0 3
#define P1 2
#define P2 1
#define P3 0

#define CL4(a, b, c, d) "v" #a, "v" #b, "v" #c, "v" #d
#define CL10(a) "v" STR(a##0), "v" STR(a##1), "v" STR(a##2), "v" STR(a##3), "v" STR(a##4), "v" STR(a##5), "v" STR(a##6), "v" STR(a##7), "v" STR(a##8), "v" STR(a##9)
__device__ __forceinline__ void att_loop(u32x4 q, unsigned ones, unsigned lane, f32x16& oo, unsigned& c0) {
  unsigned cnt = 8, nx = L_R0 - 16384;
  asm volatile(
      "v_mov_b32 v48, %[ones]\n\tv_mov_b32 v49, %[ones]\n\tv_mov_b32 v60, 0xdddd\n\t"
      "v_add_u32 v61, 0x24000, v63\n\tv_add_u32 v62, 0x20000, v63\n\t"
      "v_mov_b64 v[50:51], v[48:49]\n\tv_mov_b64 v[52:53], v[48:49]\n\tv_mov_b64 v[54:55], v[48:49]\n\tv_mov_b64 v[56:57], v[48:49]\n\tv_mov_b64 v[58:59], v[48:49]\n\t"
      ".set zr%=, 104\n\t.rept 12\n\tv_mov_b64 v[zr%=:zr%=+1], 0\n\t.set zr%=, zr%=+2\n\t.endr\n\t"
      "s_barrier\n\t"
      "ds_read_b128 v[96:99], v61\n\t"
      "ds_read_b128 v[44:47], v61 offset:2048\n\t"
      VX("v[52:55]", "v61", 1024, 1280)
      "s_waitcnt lgkmcnt(3)\n\t"
      "v_mfma_f32_32x32x16_bf16 v[80:95], v[96:99], v[40:43], 0\n\t"
      "s_nop 15\n\t"
      "v_max3_f32 v64, v80, v81, v82\n\tv_max3_f32 v65, v83, v84, v85\n\tv_max3_f32 v66, v86, v87, v88\n\t"
      "v_max3_f32 v67, v89, v90, v91\n\tv_max3_f32 v68, v92, v93, v94\n\tv_max3_f32 v64, v64, v65, v95\n\t"
      "v_max3_f32 v66, v66, v67, v68\n\tv_max_f32 v64, v64, v66\n\tv_mov_b32 v65, v64\n\t"
      "s_nop 1\n\t"
      "v_permlane32_swap_b32 v64, v65\n\t"
      "s_nop 1\n\t"
      "v_min_f32_e64 v64, -v64, -v65\n\t"
      "v_mov_b32 v65, v64\n\t"
      "v_mov_b64 v[66:67], v[64:65]\n\tv_mov_b64 v[68:69], v[64:65]\n\tv_mov_b64 v[70:71], v[64:65]\n\tv_mov_b64 v[72:73], v[64:65]\n\t"
      "v_mov_b64 v[74:75], v[64:65]\n\tv_mov_b64 v[76:77], v[64:65]\n\tv_mov_b64 v[78:79], v[64:65]\n\t"
      "s_nop 1\n\t"
      "v_mfma_f32_32x32x16_bf16 v[80:95], v[96:99], v[40:43], v[64:79]\n\t"
      "s_nop 15\n\t"
      "LOOP%=:\n\t"
      T32E(P0, "v61", 4096, "v61", 3072, 3328) T32O(P1, "v61", 6144, "v61", 5120, 5376)
      T32E(P2, "v61", 8192, "v61", 7168, 7424) T32O(P3, "v61", 10240, "v61", 9216, 9472)
      T32E(P0, "v61", 12288, "v61", 11264, 11520)
      "ds_read_b32 %[c0], v61\n\t"
      T32O(P1, "v61", 14336, "v61", 13312, 13568)
      "s_barrier\n\t"
      T32E(P2, "v62", 0, "v61", 15360, 15616) T32O(P3, "v62", 2048, "v62", 1024, 1280)
      "v_mov_b32 v61, v62\n\t"
      "s_sub_u32 %[nx], %[nx], 0x4000\n\t"
      "s_cmp_lt_u32 %[nx], 0x1c000\n\t"
      "s_cselect_b32 %[nx], 0x24000, %[nx]\n\t"
      "s_sub_u32 %[cnt], %[cnt], 1\n\t"
      "v_add_u32 v62, %[nx], v63\n\t"
      "s_cmp_lg_u32 %[cnt], 0\n\t"
      "s_cbranch_scc1 LOOP%=\n\t"
      SM("v[56:59]", "v[104:111]")
      "s_waitcnt lgkmcnt(0)\n\t"
      "s_nop 15\n\ts_nop 7\n\t"
      : [c0] "=&v"(c0), "=&{v[112:127]}"(oo), [cnt] "+s"(cnt), [nx] "+s"(nx)
      : "{v[40:43]}"(q), "{v63}"(lane), [ones] "v"(ones), [mask] "s"(VMASK)
      : "memory", "scc", CL4(44, 45, 46, 47), "v48", "v49", CL10(5), "v60", "v61", "v62", CL4(64, 65, 66, 67), "v68", "v69", CL10(7), CL10(8), CL10(9), CL10(10), "v110", "v111");
}

#define SLP 12
#define NA0 2
#define NA1 5
#define ABAR 0xAA

__global__ __launch_bounds__(1024, 4) void k_fused(const u16* __restrict__ x, const u16* __restrict__ wqkv,
                                                   const u16* __restrict__ woa, const u16* __restrict__ wq,
                                                   const u16* __restrict__ wk, const u16* __restrict__ wv,
                                                   const u16* __restrict__ wob, const u16* __restrict__ gw,
                                                   char* ws, u16* __restrict__ out) {
  __shared__ __attribute__((aligned(1024))) char smem[163840];
  const unsigned sb = (unsigned)(size_t)(LDS_AS char*)smem;
  lds_cptr S = (lds_cptr)smem;
  lds_ptr SW = (lds_ptr)smem;
  const int b = blockIdx.x, xcd = b & 7, jj = b >> 3;
  const int tb = 8 * (xcd >> 1) + (jj >> 2), hg = 4 * (xcd & 1) + (jj & 3);
  const int T0 = 64 * tb;
  const int rot = (tb + 2 * (jj & 3)) & 7;
  const int w = __builtin_amdgcn_readfirstlane(threadIdx.x >> 6);
#define LANE_VARS int l = threadIdx.x; asm volatile("" : "+v"(l)); l &= 63; \
  const int g = l >> 4, t = l & 15, q4 = t >> 2, p4 = l & 3, fsw = ((q4 >> 1) & 1) | ((g & 1) << 1)
#define WVO (((l & 7) ^ ((g & 1) << 1)) * 16)
#define RD_TAG(ep) (((u64)(unsigned)__builtin_amdgcn_readfirstlane(ep[1]) << 32) | (unsigned)__builtin_amdgcn_readfirstlane(ep[0]))

  if (w >= 8) {
    LANE_VARS;
    const int hd = w & 3, tt = (w >> 2) & 1, n = l & 31, cw = w - 8;
    __builtin_amdgcn_s_setprio(2);
    {
      const int r = 8 * cw + (l >> 3);
      const unsigned xv = (T0 + 2 * (r & 31) + (r >> 5)) * 1024 + (((l & 7) ^ ((r >> 1) & 7)) * 16);
      const unsigned wv_ = r * 1024 + (WVO ^ ((cw & 1) * 64));
#pragma unroll
      for (int i = 0; i < 4; ++i) {
        const int kc = (rot + i) & 7;
        dma2(xv, (const char*)x + kc * 128, sb + i * 8192 + cw * 1024, wv_, (const char*)wq + 128 * hg + kc * 65536, sb + L_WQ + i * 8192 + cw * 1024);
      }
    }

    const int wa = L_WQ + (8 * g + q4) * 128 + ((hd ^ fsw) * 32) + p4 * 8;
    const int xa = (32 * tt + t) * 128 + ((g ^ ((t >> 1) & 7)) * 16);
    f32x4 aq[2] = {};
    auto quarter = [&](int h) {
      bf16x8 wf[4], x0[4], x1[4];
#pragma unroll
      for (int i = 0; i < 4; ++i) {
        const int s = 4 * h + i;
        lds_cptr xp = S + (xa ^ ((s & 1) * 64)) + (s >> 1) * 8192;
        wf[i] = tr_frag(S + wa + s * 4096, 512); x0[i] = lds_b128(xp); x1[i] = lds_b128(xp + 2048);
      }
      __builtin_amdgcn_sched_barrier(0);
#pragma unroll
      for (int i = 0; i < 4; ++i) { aq[0] = MFMA16(wf[i], x0[i], aq[0]); aq[1] = MFMA16(wf[i], x1[i], aq[1]); }
    };
    WAIT_BAR(4);

    u32x2 ep = *(const LDS_AS u32x2*)(S + L_TAG);
    quarter(0);
    asm volatile("s_waitcnt vmcnt(0) lgkmcnt(0)\n\ts_barrier" : "+v"(ep) :: "memory");

    quarter(1);
    BAR();

    quarter(2);
    BAR();

    quarter(3);
    const u64 tag = RD_TAG(ep);
    const u32x4 qf = join16(pack4(aq[0] * QSCALE), pack4(aq[1] * QSCALE));
    const unsigned ones = ((l & 27) == 16) ? 0x3f803f80u : 0u;
    const u32x4 one4 = {ones, ones, ones, ones};
    f32x16 oo; unsigned c0;

    att_loop(qf, ones, sb + l * 16, oo, c0);

    bool ok = oo[8] < 1.2676506e30f;
#pragma unroll
    for (int i = 0; i < 8; ++i) ok = ok && (fabsf(oo[i]) < 3.0e38f);
    if (__builtin_amdgcn_ballot_w64(!ok) != 0) {
      const bf16x8 qq = __builtin_bit_cast(bf16x8, qf);
      float m = -__builtin_inff();
#pragma unroll 1
      for (int kt = 0; kt < 64; ++kt) {
        bf16x8 kf;
        asm volatile("global_load_dwordx4 %0, %1, %2 sc1\n\ts_waitcnt vmcnt(0)" : "=&v"(kf) : "v"(WS_KV + kt * 2048 + l * 16), "s"(ws) : "memory");
        m = fmx(m, max16(MFMA32(kf, qq, f32x16{0})));
      }
      m = xmax32(m);
      oo = f32x16{0};
#pragma unroll 1
      for (int kt = 0; kt < 64; ++kt) {
        bf16x8 kf; u32x4 v0, v1;
        asm volatile("global_load_dwordx4 %0, %3, %4 sc1\n\tglobal_load_dwordx4 %1, %3, %4 offset:1024 sc1\n\t"
                     "global_load_dwordx4 %2, %3, %4 offset:1280 sc1\n\ts_waitcnt vmcnt(0)"
                     : "=&v"(kf), "=&v"(v0), "=&v"(v1) : "v"(WS_KV + kt * 2048 + l * 16), "s"(ws) : "memory");
        f32x16 s = MFMA32(kf, qq, f32x16{0});
        f32x8 p0, p1;
#pragma unroll
        for (int i = 0; i < 8; ++i) { p0[i] = __builtin_amdgcn_exp2f(s[i] - m); p1[i] = __builtin_amdgcn_exp2f(s[8 + i] - m); }
        if (n >= 16) { v0 = one4; v1 = one4; }
        oo = __builtin_amdgcn_smfmac_f32_32x32x32_bf16(__builtin_bit_cast(bf16x8, v0), __builtin_bit_cast(bf16x16, p0), oo, 0xdddd, 0, 0);
        oo = __builtin_amdgcn_smfmac_f32_32x32x32_bf16(__builtin_bit_cast(bf16x8, v1), __builtin_bit_cast(bf16x16, p1), oo, 0xdddd, 0, 0);
      }
    }
    {
      float f = __builtin_amdgcn_rcpf(oo[8]);
      bf16x8 ob;
#pragma unroll
      for (int j = 0; j < 8; ++j) ob[j] = (__bf16)(oo[j] * f);
      gst16_sc1(WS_O + ((2 * tb + tt) * 64 + 32 + 4 * hg + hd) * 1024 + l * 16, ws, __builtin_bit_cast(u32x4, ob));
    }
    if (arrive(sb + L_R0 - 16384, c0) == 7) put_flag(ws + WS_FB + (tb * 8 + hg) * 256, tag);

    return;
  }

  u64 tag = 0;
  const int tbo = 4 * xcd + (jj >> 3), cg = jj & 7;
  if (w < 4) {
    __builtin_amdgcn_s_setprio(3);
    LANE_VARS;
    u32x2 ep;
    const int tk = T0 + 16 * (hg >> 1);
    const unsigned kxo = (unsigned)((tk + t) * 1024 + 256 * w + 16 * g);
    u32x4 kx0 = gld16<0>(kxo, x), kx1 = gld16<64>(kxo, x), kx2 = gld16<128>(kxo, x), kx3 = gld16<192>(kxo, x);
    {
      const unsigned kv = (8 * ((l >> 3) & 3) + 4 * (l >> 5) + ((l >> 1) & 3)) * 32 + (l & 1) * 16;
      dma4(kv, kv + 1024, kv + 2048, kv + 3072, (const char*)((hg & 1) ? wv : wk) + 4096 * w, sb + L_R0 + 4096 * w);
    }
    const unsigned gwo = 256 * w + 16 * g;
    u32x4 g0, g1, g2, g3;
    asm volatile("s_mov_b64 exec, %6\n\tglobal_load_dwordx4 %0, %4, %5\n\tglobal_load_dwordx4 %1, %4, %5 offset:64\n\t"
                 "global_load_dwordx4 %2, %4, %5 offset:128\n\tglobal_load_dwordx4 %3, %4, %5 offset:192\n\ts_mov_b64 exec, -1"
                 : "=&v"(g0), "=&v"(g1), "=&v"(g2), "=&v"(g3) : "v"(gwo), "s"(gw), "s"(0x0001000100010001ull) : "memory");
    asm volatile("global_load_dwordx2 %0, %1, %2 sc1" : "=v"(ep) : "v"(0u), "s"(ws + WS_EPOCH) : "memory");
    const int tp = (t & 3) | ((t & 4) << 1) | ((t & 8) >> 1);
    unsigned ko = (tk >> 5) * 2048 + (g >> 1) * 512 + ((hg >> 1) & 1) * 256 + (g & 1) * 8 + ((hg & 1) ? 1024 + tp * 16 : t * 16);
    asm volatile("s_waitcnt vmcnt(0)" : "+v"(ep), "+v"(kx0), "+v"(kx1), "+v"(kx2), "+v"(kx3), "+v"(g0), "+v"(g1), "+v"(g2), "+v"(g3), "+v"(ko) :: "memory");

    f32x4 d = {0, 0, 0, 0}, dg = {0, 0, 0, 0};
    {
      lds_cptr wp = S + L_R0 + 4096 * w + l * 8;
      const bf16x8 xa[4] = {__builtin_bit_cast(bf16x8, kx0), __builtin_bit_cast(bf16x8, kx1), __builtin_bit_cast(bf16x8, kx2), __builtin_bit_cast(bf16x8, kx3)};
      const bf16x8 ga[4] = {__builtin_bit_cast(bf16x8, g0), __builtin_bit_cast(bf16x8, g1), __builtin_bit_cast(bf16x8, g2), __builtin_bit_cast(bf16x8, g3)};
      bf16x8 wf[4];
#pragma unroll
      for (int i = 0; i < 4; ++i) wf[i] = tr_frag(wp + 1024 * i, 512);
      if (hg & 1) {
#pragma unroll
        for (int i = 0; i < 4; ++i) { dg = MFMA16(ga[i], xa[i], dg); d = MFMA16(xa[i], wf[i], d); }
      } else {
#pragma unroll
        for (int i = 0; i < 4; ++i) d = MFMA16(wf[i], xa[i], d);
      }
    }
    *(LDS_AS f32x4*)(SW + L_R0 + 4096 * w + l * 16) = d;
    *(LDS_AS float*)(SW + L_R0 + 4096 * w + 1024 + l * 4) = dg[0];
    tag = RD_TAG(ep) * 6364136223846793005ull + 1442695040888963407ull;
    *(LDS_AS u64*)(SW + L_TAG + 4096 * w) = tag;

    LBAR();

    if (w == 0) {
#pragma unroll
      for (int i = 1; i < 4; ++i) d += *(const LDS_AS f32x4*)(S + L_R0 + 4096 * i + l * 16);
      const u32x2 kv = __builtin_bit_cast(u32x2, pack4(d));
      gst16_sc1(2 * ko, ws + WS_KG, u32x4{kv[0], (unsigned)tag, kv[1], (unsigned)tag});
      gst8_sc1(ko, ws + WS_KV, kv);
    } else if (w == 1 && (hg & 1)) {
      float dz = 0;
#pragma unroll
      for (int i = 0; i < 4; ++i) dz += *(const LDS_AS float*)(S + L_R0 + 4096 * i + 1024 + l * 4);
      if (l < 16) gst4_sc1(WS_G + (tk + l) * 4, ws, __float_as_uint(__builtin_amdgcn_rcpf(1.f + __expf(-dz))));
    }

    const unsigned fo = WS_FKV + (2 * w + ((l >> 2) & 1)) * 256 + p4 * 8, vo = WS_KV + w * 4096 + l * 16;
    unsigned o0 = (l >> 3) * 1024 + WVO;
    int ct = 16 * (xcd >> 1), slot = L_R0;
    {
      const unsigned go = w * 8192 + l * 16;
      const unsigned la0 = sb + w * 4096 + l * 8;
      f32x32 a;
#define GLD(R, O, P) "global_load_dwordx4 v[" STR(R) ":" STR(R) "+3], %[go], " P " offset:" STR(O) " sc1\n\t"
#define SWEEP(P) GLD(96, -4096, P) GLD(100, -3072, P) GLD(104, -2048, P) GLD(108, -1024, P) GLD(112, 0, P) GLD(116, 1024, P) GLD(120, 2048, P) GLD(124, 3072, P)
#define GCK(D, R) "v_bitop3_b32 " D ", v[" STR(R) "+1], v[" STR(R) "+3], %[tag] bitop3:0x7e\n\t"
#define GW2(A, R) "ds_write2_b32 " A ", v[" STR(R) "], v[" STR(R) "+2] offset1:1\n\tds_write2_b32 " A ", v[" STR(R) "+4], v[" STR(R) "+6] offset0:128 offset1:129\n\t"
      const char* gp = ws + WS_KG + 4096 + ct * 4096;
      LBAR();

      BAR();

      asm volatile(SWEEP("%[gp]") "s_barrier" : "={v[96:127]}"(a) : [go] "v"(go), [gp] "s"(gp) : "memory");
#pragma unroll 1
      for (int c = 0; c < 2; ++c) {
        const char* gn = gp + 32768;
        unsigned t0, t1, t2, t3, it = 0;
        asm volatile(
            "GS%=:\n\ts_waitcnt vmcnt(0)\n\t"
            GCK("%[t0]", 96) GCK("%[t1]", 100) GCK("%[t2]", 104) GCK("%[t3]", 108) "v_or3_b32 %[t0], %[t0], %[t1], %[t2]\n\t"
            GCK("%[t1]", 112) GCK("%[t2]", 116) "v_or3_b32 %[t0], %[t0], %[t3], %[t1]\n\t"
            GCK("%[t3]", 120) GCK("%[t1]", 124) "v_or3_b32 %[t0], %[t0], %[t2], %[t3]\n\tv_or_b32 %[t0], %[t0], %[t1]\n\t"
            "v_cmp_ne_u32 vcc, 0, %[t0]\n\t"
            "s_add_u32 %[it], %[it], 1\n\t"
            "s_cbranch_vccz GW%=\n\t"
            "s_cmp_gt_u32 %[it], 0x40000\n\t"
            "s_cbranch_scc1 GW%=\n\t"
            SWEEP("%[gp]")
            "s_branch GS%=\n\t"
            "GW%=:\n\t"
            "v_add_u32 %[t0], %[slot], %[la]\n\tv_add_u32 %[t1], 0x400, %[t0]\n\tv_add_u32 %[t2], 0x800, %[t0]\n\tv_add_u32 %[t3], 0xc00, %[t0]\n\t"
            GW2("%[t0]", 96) GW2("%[t1]", 104) GW2("%[t2]", 112) GW2("%[t3]", 120)
            "s_cmp_eq_u32 %[c], 1\n\t"
            "s_cbranch_scc1 GE%=\n\t"
            SWEEP("%[gn]")
            "GE%=:"
            : "+{v[96:127]}"(a), [t0] "=&v"(t0), [t1] "=&v"(t1), [t2] "=&v"(t2), [t3] "=&v"(t3), [it] "+s"(it)
            : [go] "v"(go), [gp] "s"(gp), [gn] "s"(gn), [tag] "s"((unsigned)tag), [la] "v"(la0), [slot] "s"(slot), [c] "s"(c) : "memory", "scc", "vcc");

        gp = gn; slot -= 16384;
        if (c == 1 && w == 0) put_flag(ws + WS_FKV + (tk >> 5) * 256 + (hg & 3) * 8, tag);
        LBAR();

      }
      ct = (ct + 16) & 63;
    }
#pragma unroll 1
    for (int c = 2; c < 8; ++c) {
      poll_flags(fo, ws + ct * 256, tag);

      const char* kp = ws + ct * 2048;
      u32x4 a0 = gld16_sc1<0>(vo, kp), a1 = gld16_sc1<1024>(vo, kp), a2 = gld16_sc1<2048>(vo, kp), a3 = gld16_sc1<3072>(vo, kp);
      asm volatile("s_waitcnt vmcnt(0)" : "+v"(a0), "+v"(a1), "+v"(a2), "+v"(a3) :: "memory");
      lds_ptr dp = SW + slot + w * 4096 + l * 16;
      *(LDS_AS u32x4*)dp = a0; *(LDS_AS u32x4*)(dp + 1024) = a1; *(LDS_AS u32x4*)(dp + 2048) = a2; *(LDS_AS u32x4*)(dp + 3072) = a3;
      if (c >= 6) {
        const char* p = (const char*)(c == 6 ? woa : wob) + 128 * cg + w * 32768;
        const unsigned dst = sb + (c & 1) * 65536 + w * 4096;
#pragma unroll
        for (int i = 0; i < 4; ++i)
          if (i < 3 || c == 6) dma4(o0, (o0 ^ 64) + 8192, o0 + 16384, (o0 ^ 64) + 24576, p + i * 131072, dst + i * 16384);
      }
      ct = (ct + 8) & 63; slot = slot == L_S2 ? L_R0 : slot - 16384;

      LBAR();

    }

    __builtin_amdgcn_s_setprio(0);
    if (b == 0 && w == 0) put_flag(ws + WS_EPOCH, tag);
  } else {
    LANE_VARS;
    const int hd = w & 3;
    {
      const int r = 16 * hd + (l >> 3);
      const unsigned xv = (T0 + 2 * (r & 31) + (r >> 5)) * 1024 + (((l & 7) ^ ((r >> 1) & 7)) * 16), xv1 = (xv ^ 64) + 16384;
      const unsigned wv_ = r * 1024 + WVO, wv1 = (wv_ ^ 64) + 8192;
#pragma unroll
      for (int i = 0; i < 8; ++i) {
        const int kc = (rot + 4 + (i >> 1)) & 7, lo = (4 + (i >> 1)) * 8192 + hd * 2048 + (i & 1) * 1024;
        if (i == NA0) { BAR(); }
        if (i == NA1) { u32x2 ep = *(const LDS_AS u32x2*)(S + L_TAG); tag = RD_TAG(ep); BAR(); }
        dma2((i & 1) ? xv1 : xv, (const char*)x + kc * 128, sb + lo, (i & 1) ? wv1 : wv_, (const char*)wq + 128 * hg + kc * 65536, sb + L_WQ + lo);
      }
    }
    unsigned wvo = (8 * ((l >> 3) & 3) + 4 * (l >> 5) + ((l >> 1) & 3)) * 3072 + (l & 1) * 16;
    const char* wsrc = (const char*)wqkv + (4 * hg + hd) * 32;
    const int ring = L_WQ + hd * 6144;
    const int th = T0 - 16 + 2 * (t & 7);
    unsigned hvo = ((th < 0 ? 0 : th) + (t >> 3)) * 1024 + g * 16;
    int ks = 2 * rot;
    bf16x8 hx[2] = {};
    auto stage_w = [&](int sl) {
      dma3(wvo, wsrc + ks * 98304, sb + ring + sl * 3072);
      asm volatile("global_load_dwordx4 %0, %1, %2" : "+v"(hx[sl]) : "v"(hvo), "s"((const char*)x + ks * 64) : "memory");
      ks = (ks + 1) & 15;
    };
    int xa = t * 128 + ((g ^ ((t >> 1) & 7)) * 16);
    f32x4 aa[4] = {}, ak[5] = {}, av[5] = {};
    f32x4 mo, mp, mh[2];
#pragma unroll
    for (int r = 0; r < 4; ++r) {
      mo[r] = ((unsigned)(t - 4 * g - r) <= 7u) ? 0.f : -__builtin_inff();
      mp[r] = (4 * g + r >= t + 9) ? 0.f : -__builtin_inff();
      mh[1][r] = tb > 0 ? mp[r] : -__builtin_inff();
      mh[0][r] = (tb > 0 && g < 2 && 4 * g + r > t) ? 0.f : -__builtin_inff();
    }
    int wr = ring + l * 8;
    asm volatile("" : "+v"(aa[0]), "+v"(aa[1]), "+v"(aa[2]), "+v"(aa[3]), "+v"(ak[0]), "+v"(ak[1]), "+v"(ak[2]), "+v"(ak[3]), "+v"(ak[4]),
                      "+v"(av[0]), "+v"(av[1]), "+v"(av[2]), "+v"(av[3]), "+v"(av[4]), "+v"(mo), "+v"(mp), "+v"(mh[0]), "+v"(mh[1]),
                      "+v"(hx[0]), "+v"(hx[1]), "+v"(xa), "+v"(wr), "+v"(wvo), "+v"(hvo));

    WAIT_BAR(8);
    WAIT_BAR(0);

    BAR();
    stage_w(0); stage_w(1);
    BAR();
#pragma unroll 1
    for (int j = 0; j < 8; ++j) {
#pragma unroll
      for (int h = 0; h < 2; ++h) {
        bf16x8 wf[3], xf[4];
#pragma unroll
        for (int i = 0; i < 4; ++i) xf[i] = lds_b128(S + (xa ^ (h * 64)) + i * 2048);
        asm volatile("s_waitcnt vmcnt(4)" : "+v"(hx[h]) :: "memory");
#pragma unroll
        for (int m = 0; m < 3; ++m) wf[m] = tr_frag(S + wr + h * 3072 + m * 1024, 512);
        asm volatile("s_waitcnt lgkmcnt(0)" : "+v"(wf[0]), "+v"(wf[1]), "+v"(wf[2]) :: "memory");
        ak[4] = MFMA16(wf[1], hx[h], ak[4]); av[4] = MFMA16(hx[h], wf[2], av[4]);
        asm volatile("" : "+v"(ak[4]), "+v"(av[4]));
        stage_w(h);
#pragma unroll
        for (int i = 0; i < 4; ++i) {
          aa[i] = MFMA16(wf[0], xf[i], aa[i]);
          ak[i] = MFMA16(wf[1], xf[i], ak[i]); av[i] = MFMA16(xf[i], wf[2], av[i]);
        }
      }
      xa += 8192;

      if ((ABAR >> j) & 1) { BAR(); }
    }
    asm volatile("s_waitcnt vmcnt(0)" : "+v"(hx[0]), "+v"(hx[1]) :: "memory");

    const s16x4 kh_ = pack4(ak[4]), vh_ = pack4(av[4]);
#pragma unroll
    for (int tt = 0; tt < 2; ++tt) {
      s16x4 kk[3] = {kh_, pack4(ak[2 * tt]), pack4(ak[2 * tt + 1])}, vv[3] = {vh_, pack4(av[2 * tt]), pack4(av[2 * tt + 1])};
      const s16x4 on = {0x3f80, 0x3f80, 0x3f80, 0x3f80};
      s16x4 ob[2], po[2], pp[2];
      f32x4 so[2], sp[2], o[2], ls[2];
      float m[2];
      const f32x4 z = {0, 0, 0, 0};
#pragma unroll
      for (int i = 0; i < 2; ++i) {
        s16x4 q = pack4(aa[2 * tt + i] * QSCALE);
        so[i] = MFMA16K(kk[i + 1], q, mo); sp[i] = MFMA16K(kk[i], q, i ? mp : mh[tt]);
      }
#pragma unroll
      for (int i = 0; i < 2; ++i) m[i] = fmx(fmx(fmx(fmx(so[i][0], so[i][1]), so[i][2]), fmx(fmx(so[i][3], sp[i][0]), sp[i][1])), fmx(sp[i][2], sp[i][3]));
      {
        auto r = __builtin_amdgcn_permlane16_swap(__float_as_uint(m[0]), __float_as_uint(m[1]), false, false);
        float y = xmax32(fmx(__uint_as_float(r[0]), __uint_as_float(r[1])));
        auto u = __builtin_amdgcn_permlane16_swap(__float_as_uint(y), __float_as_uint(y), false, false);
        m[0] = __uint_as_float(u[0]); m[1] = __uint_as_float(u[1]);
      }
#pragma unroll
      for (int i = 0; i < 2; ++i) {
#pragma unroll
        for (int r = 0; r < 4; ++r) { so[i][r] = __builtin_amdgcn_exp2f(so[i][r] - m[i]); sp[i][r] = __builtin_amdgcn_exp2f(sp[i][r] - m[i]); }
        po[i] = pack4(so[i]); pp[i] = pack4(sp[i]);
        ls[i] = MFMA16K(on, pp[i], MFMA16K(on, po[i], z));
        o[i] = MFMA16K(vv[i], pp[i], MFMA16K(vv[i + 1], po[i], z));
      }
#pragma unroll
      for (int i = 0; i < 2; ++i) ob[i] = pack4(o[i] * __builtin_amdgcn_rcpf(ls[i][0]));
      gst16_sc1(WS_O + ((2 * tb + tt) * 64 + 4 * hg + hd) * 1024 + l * 16, ws, join16(ob[0], ob[1]));

    }
    unsigned c0 = *(const LDS_AS unsigned*)(S + L_S2);
    asm volatile("s_waitcnt lgkmcnt(0)\n\ts_barrier" : "+v"(c0) :: "memory");

    if (arrive(sb + L_S2, c0) == 3) put_flag(ws + WS_FA + (tb * 8 + hg) * 256, tag);

    unsigned o0 = (l >> 3) * 1024 + WVO, o1 = (o0 ^ 64) + 8192, o2 = o0 + 16384, o3 = o1 + 16384;
    asm volatile("s_barrier" : "+v"(o0), "+v"(o1), "+v"(o2), "+v"(o3) :: "memory");

    dma4(o0, o1, o2, o3, (const char*)wob + 128 * cg + hd * 32768 + 393216, sb + L_S2 + hd * 4096);

  }

  LANE_VARS;
  const int to = (w >> 1) & 1, hf = w & 1, kh = w >> 2;
  const unsigned ovo = WS_O + ((2 * tbo + to) * 64 + 16 * kh + (g >> 1)) * 1024 + ((g & 1) * 32 + 16 * hf + t) * 16;
  const int tok = 64 * tbo + 2 * (16 * hf + t) + to;
  const unsigned oso = tok * 1024 + 128 * cg + 64 * kh + 32 * (g & 1) + 16 * (g >> 1);
  int wo = ((8 * g + q4) * 128 + fsw * 32 + p4 * 8 + kh * 32768) ^ (64 * kh);
  const int rb = L_R0 + w * 2048 + l * 16;
  unsigned fo = tbo * 2048 + (l & 7) * 256;
  f32x4 keep[4] = {};
  const char* fl = ws + WS_FA;
  const char* ob = ws;
#pragma unroll 1
  for (int pass = 0;; ++pass) {
    asm volatile("" : "+s"(pass));
    bf16x8 wf[4][4];
#pragma unroll
    for (int j = 0; j < 4; ++j)
#pragma unroll
      for (int c = 0; c < 4; ++c) wf[j][c] = tr_frag(S + (wo ^ (32 * c)) + j * 4096, 512);
    poll_flags(fo, fl, tag);

    u32x4 of[8];
    float gs;
    of[0] = gld16_sc1<0>(ovo, ob); of[1] = gld16_sc1<2048>(ovo, ob); of[2] = gld16_sc1<0>(ovo, ob + 4096); of[3] = gld16_sc1<2048>(ovo, ob + 4096);
    of[4] = gld16_sc1<0>(ovo, ob + 8192); of[5] = gld16_sc1<2048>(ovo, ob + 8192); of[6] = gld16_sc1<0>(ovo, ob + 12288); of[7] = gld16_sc1<2048>(ovo, ob + 12288);
    asm volatile("global_load_dword %0, %1, %2 sc1" : "=v"(gs) : "v"(WS_G + tok * 4), "s"(ws) : "memory");
    f32x4 acc[4] = {};
    if (!pass) { BAR(); __builtin_amdgcn_s_sleep(SLP); }
    asm volatile("s_waitcnt vmcnt(5)" : "+v"(of[0]), "+v"(of[1]), "+v"(of[2]), "+v"(of[3]) :: "memory");

#pragma unroll
    for (int j = 0; j < 4; ++j)
#pragma unroll
      for (int c = 0; c < 4; ++c) {
        acc[c] = MFMA16(wf[j][c], __builtin_bit_cast(bf16x8, of[j]), acc[c]);
        wf[j][c] = tr_frag(S + (wo ^ (32 * c)) + (4 + j) * 4096, 512);
      }
    asm volatile("s_waitcnt vmcnt(0)" : "+v"(of[4]), "+v"(of[5]), "+v"(of[6]), "+v"(of[7]), "+v"(gs) :: "memory");
#pragma unroll
    for (int j = 0; j < 4; ++j)
#pragma unroll
      for (int c = 0; c < 4; ++c) acc[c] = MFMA16(wf[j][c], __builtin_bit_cast(bf16x8, of[4 + j]), acc[c]);

    if (pass) gs = 1.f - gs;
#pragma unroll
    for (int c = 3; c >= 0; --c) keep[c] += acc[c] * gs;
    if (pass) {
      *(LDS_AS f32x4*)(SW + rb) = keep[2];
      *(LDS_AS f32x4*)(SW + rb + 1024) = keep[3];
      LBAR();
    }
    lds_cptr pr = S + (rb ^ 8192);
    const f32x4 r0 = keep[0] + *(const LDS_AS f32x4*)(pr), r1 = keep[1] + *(const LDS_AS f32x4*)(pr + 1024);
    *(u32x4*)((char*)out + oso) = join16(pack4(r0), pack4(r1));
    if (pass) break;

    fl += WS_FB - WS_FA; ob += 32768; wo += 65536; fo = (fo & ~0x400u) | (kh << 10);
  }

}

extern "C" void kernel_launch(void* const* d_in, const int* in_sizes, int n_in, void* d_out, int out_size, void* d_ws,
                              size_t ws_size, hipStream_t stream) {
  k_fused<<<256, 1024, 0, stream>>>((const u16*)d_in[0], (const u16*)d_in[1], (const u16*)d_in[2], (const u16*)d_in[4],
                                    (const u16*)d_in[5], (const u16*)d_in[6], (const u16*)d_in[7], (const u16*)d_in[8],
                                    (char*)d_ws, (u16*)d_out);
}
```

```cpp
#include <hip/hip_runtime.h>
#include <hip/hip_bf16.h>

typedef unsigned short u16;
typedef unsigned long long u64;
typedef __attribute__((ext_vector_type(2))) float f32x2;
typedef __attribute__((ext_vector_type(2))) __bf16 bf16x2;
typedef __attribute__((ext_vector_type(4))) float f32x4;
typedef __attribute__((ext_vector_type(8))) float f32x8;
typedef __attribute__((ext_vector_type(16))) float f32x16;
typedef __attribute__((ext_vector_type(32))) float f32x32;
typedef __attribute__((ext_vector_type(16))) __bf16 bf16x16;
typedef __attribute__((ext_vector_type(8))) __bf16 bf16x8;
typedef __attribute__((ext_vector_type(4))) __bf16 bf16x4;
typedef __attribute__((ext_vector_type(4))) short s16x4;
typedef __attribute__((ext_vector_type(8))) short s16x8;
typedef __attribute__((ext_vector_type(4))) unsigned u32x4;
typedef __attribute__((ext_vector_type(2))) unsigned u32x2;
typedef __attribute__((ext_vector_type(8))) unsigned u32x8;
typedef __attribute__((ext_vector_type(16))) unsigned u32x16;
#define LDS_AS __attribute__((address_space(3)))
typedef LDS_AS const char* lds_cptr;
typedef LDS_AS char* lds_ptr;

#define MFMA32(a, b, c) __builtin_amdgcn_mfma_f32_32x32x16_bf16(a, b, c, 0, 0, 0)
#define MFMA16(a, b, c) __builtin_amdgcn_mfma_f32_16x16x32_bf16(a, b, c, 0, 0, 0)
#define MFMA16K(a, b, c) __builtin_amdgcn_mfma_f32_16x16x16bf16_1k(a, b, c, 0, 0, 0)
#define WAIT_BAR(N) asm volatile("s_waitcnt vmcnt(" #N ") lgkmcnt(0)\n\ts_barrier" ::: "memory")
#define LBAR() asm volatile("s_waitcnt lgkmcnt(0)\n\ts_barrier" ::: "memory")
#define BAR() asm volatile("s_barrier" ::: "memory")
#define STR2(x) #x
#define STR(x) STR2(x)

constexpr unsigned WS_EPOCH = 0;
constexpr unsigned WS_FKV = 4096;
constexpr unsigned WS_G = 32768;
constexpr unsigned WS_KV = 65536;
constexpr unsigned WS_FA = 262144;
constexpr unsigned WS_FB = 327680;
constexpr unsigned WS_KG = 524288;
constexpr unsigned WS_O = 1u << 20;

constexpr float QSCALE = 0.25f * 1.4426950408889634f;

__device__ __forceinline__ void dma4(unsigned v0, unsigned v1, unsigned v2, unsigned v3, const void* sbase, unsigned lds) {
  unsigned sv;
  asm volatile("s_mov_b32 %0, m0\n\ts_mov_b32 m0, %6\n\ts_nop 0\n\tglobal_load_lds_dwordx4 %1, %5\n\t"
               "s_add_u32 m0, m0, 0x400\n\ts_nop 0\n\tglobal_load_lds_dwordx4 %2, %5\n\t"
               "s_add_u32 m0, m0, 0x400\n\ts_nop 0\n\tglobal_load_lds_dwordx4 %3, %5\n\t"
               "s_add_u32 m0, m0, 0x400\n\ts_nop 0\n\tglobal_load_lds_dwordx4 %4, %5\n\ts_mov_b32 m0, %0"
               : "=&s"(sv) : "v"(v0), "v"(v1), "v"(v2), "v"(v3), "s"(sbase), "s"(lds) : "memory", "scc");
}
__device__ __forceinline__ void dma2(unsigned v0, const void* s0, unsigned l0, unsigned v1, const void* s1, unsigned l1) {
  unsigned sv;
  asm volatile("s_mov_b32 %0, m0\n\ts_mov_b32 m0, %3\n\ts_nop 0\n\tglobal_load_lds_dwordx4 %1, %2\n\t"
               "s_mov_b32 m0, %6\n\ts_nop 0\n\tglobal_load_lds_dwordx4 %4, %5\n\ts_mov_b32 m0, %0"
               : "=&s"(sv) : "v"(v0), "s"(s0), "s"(l0), "v"(v1), "s"(s1), "s"(l1) : "memory");
}
__device__ __forceinline__ void dma3(unsigned v0, const void* s0, unsigned l0) {
  unsigned sv;
  asm volatile("s_mov_b32 %0, m0\n\ts_mov_b32 m0, %3\n\ts_nop 0\n\tglobal_load_lds_dwordx4 %1, %2\n\t"
               "global_load_lds_dwordx4 %1, %2 offset:1024\n\tglobal_load_lds_dwordx4 %1, %2 offset:2048\n\ts_mov_b32 m0, %0"
               : "=&s"(sv) : "v"(v0), "s"(s0), "s"(l0) : "memory");
}
template <int OFF> __device__ __forceinline__ u32x4 gld16(unsigned voff, const void* sbase) {
  u32x4 r;
  asm volatile("global_load_dwordx4 %0, %1, %2 offset:%3" : "=v"(r) : "v"(voff), "s"(sbase), "n"(OFF) : "memory");
  return r;
}
template <int OFF> __device__ __forceinline__ u32x4 gld16_sc1(unsigned voff, const void* sbase) {
  u32x4 r;
  asm volatile("global_load_dwordx4 %0, %1, %2 offset:%3 sc1" : "=v"(r) : "v"(voff), "s"(sbase), "n"(OFF) : "memory");
  return r;
}
__device__ __forceinline__ void gst16_sc1(unsigned voff, void* sbase, u32x4 v) {
  asm volatile("global_store_dwordx4 %0, %1, %2 sc1\n\ts_nop 1" :: "v"(voff), "v"(v), "s"(sbase) : "memory");
}
__device__ __forceinline__ void gst8_sc1(unsigned voff, void* sbase, u32x2 v) {
  asm volatile("global_store_dwordx2 %0, %1, %2 sc1\n\ts_nop 1" :: "v"(voff), "v"(v), "s"(sbase) : "memory");
}
__device__ __forceinline__ void gst4_sc1(unsigned voff, void* sbase, unsigned v) {
  asm volatile("global_store_dword %0, %1, %2 sc1\n\ts_nop 1" :: "v"(voff), "v"(v), "s"(sbase) : "memory");
}
__device__ __forceinline__ void put_flag(const void* p, u64 tag) {
  unsigned a; u64 v;
  asm volatile("s_mov_b64 exec, 1\n\tv_mov_b32 %0, 0\n\tv_mov_b64 %1, %2\n\tglobal_store_dwordx2 %0, %1, %3 sc1\n\ts_mov_b64 exec, -1\n\ts_nop 1"
               : "=&v"(a), "=&v"(v) : "s"(tag), "s"(p) : "memory");
}
__device__ __forceinline__ unsigned arrive(unsigned lds, unsigned c0) {
  unsigned old;
  asm volatile("s_waitcnt vmcnt(0)\n\ts_mov_b64 exec, 1\n\tds_add_rtn_u32 %0, %1, %2\n\ts_waitcnt lgkmcnt(0)\n\ts_mov_b64 exec, -1"
               : "=&v"(old) : "v"(lds), "v"(1u) : "memory");
  return __builtin_amdgcn_readfirstlane(old - c0);
}
__device__ __forceinline__ void poll_flags(unsigned voff, const void* sbase, u64 tag) {
#pragma unroll 1
  for (int it = 0; it < (1 << 20); ++it) {
    u64 f;
    asm volatile("global_load_dwordx2 %0, %1, %2 sc1\n\ts_waitcnt vmcnt(0)" : "=&v"(f) : "v"(voff), "s"(sbase) : "memory");
    if (__builtin_amdgcn_ballot_w64(f != tag) == 0) break;
    __builtin_amdgcn_s_sleep(2);
  }
}
__device__ __forceinline__ bf16x8 lds_b128(lds_cptr p) { return *(const LDS_AS bf16x8*)p; }
__device__ __forceinline__ s16x4 lds_tr(lds_cptr p) {
  return __builtin_amdgcn_ds_read_tr16_b64_v4i16((LDS_AS s16x4*)p);
}
__device__ __forceinline__ bf16x8 tr_frag(lds_cptr p, int off2) {
  s16x4 a = lds_tr(p), b = lds_tr(p + off2);
  s16x8 r = __builtin_shufflevector(a, b, 0, 1, 2, 3, 4, 5, 6, 7);
  return __builtin_bit_cast(bf16x8, r);
}
__device__ __forceinline__ unsigned pack2(float a, float b) {
  return __builtin_bit_cast(unsigned, __builtin_convertvector(f32x2{a, b}, bf16x2));
}
__device__ __forceinline__ s16x4 pack4(f32x4 a) {
  u32x2 r = {pack2(a[0], a[1]), pack2(a[2], a[3])};
  asm("" : "+v"(r));
  return __builtin_bit_cast(s16x4, r);
}
__device__ __forceinline__ float fmx(float a, float b) { return __builtin_elementwise_maximum(a, b); }
__device__ __forceinline__ u32x4 join16(s16x4 a, s16x4 b) {
  u32x2 x = __builtin_bit_cast(u32x2, a), y = __builtin_bit_cast(u32x2, b);
  auto r0 = __builtin_amdgcn_permlane16_swap(x[0], y[0], false, false);
  auto r1 = __builtin_amdgcn_permlane16_swap(x[1], y[1], false, false);
  return u32x4{r0[0], r1[0], r0[1], r1[1]};
}
__device__ __forceinline__ float xmax32(float v) {
  auto r = __builtin_amdgcn_permlane32_swap(__float_as_uint(v), __float_as_uint(v), false, false);
  return fmx(__uint_as_float(r[0]), __uint_as_float(r[1]));
}
__device__ __forceinline__ float xmax16(float v) {
  auto r = __builtin_amdgcn_permlane16_swap(__float_as_uint(v), __float_as_uint(v), false, false);
  return fmx(__uint_as_float(r[0]), __uint_as_float(r[1]));
}
__device__ __forceinline__ float max16(const f32x16& s) {
  float a = fmx(fmx(s[0], s[1]), s[2]);
#pragma unroll
  for (int i = 3; i < 15; i += 2) a = fmx(fmx(a, s[i]), s[i + 1]);
  return fmx(a, s[15]);
}

constexpr int L_WQ = 65536, L_R0 = 147456;
constexpr int L_TAG = L_R0 + 2048;
constexpr int L_S2 = 114688;

#define EX(r) "v_exp_f32 v" STR(r) ", v" STR(r) "\n\t"
#define E3(a, b, c) EX(a) EX(b) EX(c)
#define E5(a, b, c, d, e) EX(a) EX(b) EX(c) EX(d) EX(e)
#define SM(V, S) "v_smfmac_f32_32x32x32_bf16 v[112:127], " V ", " S ", v60\n\t"
#define MF(S, KA, KOFF) "s_waitcnt lgkmcnt(0)\n\tv_mfma_f32_32x32x16_bf16 " S ", v[44:47], v[40:43], v[64:79]\n\tds_read_b128 v[44:47], " KA " offset:" STR(KOFF) "\n\t"
#define VX(V1, VA, O0, O1) "s_mov_b64 exec, %[mask]\n\tds_read_b128 v[48:51], " VA " offset:" STR(O0) "\n\tds_read_b128 " V1 ", " VA " offset:" STR(O1) "\n\ts_mov_b64 exec, -1\n\t"
#define SP(p) "s_setprio " STR(p) "\n\t"
#define T32E(P, KA, KOFF, VA, O0, O1) E3(80, 81, 82) SP(P) SM("v[56:59]", "v[104:111]") E5(83, 84, 85, 86, 87) MF("v[96:111]", KA, KOFF) \
  E5(88, 89, 90, 91, 92) SM("v[48:51]", "v[80:87]") VX("v[56:59]", VA, O0, O1) E3(93, 94, 95)
#define T32O(P, KA, KOFF, VA, O0, O1) E3(96, 97, 98) SP(P) SM("v[52:55]", "v[88:95]") E5(99, 100, 101, 102, 103) MF("v[80:95]", KA, KOFF) \
  E5(104, 105, 106, 107, 108) SM("v[48:51]", "v[96:103]") VX("v[52:55]", VA, O0, O1) E3(109, 110, 111)
constexpr u64 VMASK = 0x0000FFFF0000FFFFull;
#define P0 3
#define P1 2
#define P2 1
#define P3 0

#define CL4(a, b, c, d) "v" #a, "v" #b, "v" #c, "v" #d
#define CL10(a) "v" STR(a##0), "v" STR(a##1), "v" STR(a##2), "v" STR(a##3), "v" STR(a##4), "v" STR(a##5), "v" STR(a##6), "v" STR(a##7), "v" STR(a##8), "v" STR(a##9)
__device__ __forceinline__ void att_loop(u32x4 q, unsigned ones, unsigned lane, f32x16& oo, unsigned& c0) {
  unsigned cnt = 8, nx = L_R0 - 16384;
  asm volatile(
      "v_mov_b32 v48, %[ones]\n\tv_mov_b32 v49, %[ones]\n\tv_mov_b32 v60, 0xdddd\n\t"
      "v_add_u32 v61, 0x24000, v63\n\tv_add_u32 v62, 0x20000, v63\n\t"
      "v_mov_b64 v[50:51], v[48:49]\n\tv_mov_b64 v[52:53], v[48:49]\n\tv_mov_b64 v[54:55], v[48:49]\n\tv_mov_b64 v[56:57], v[48:49]\n\tv_mov_b64 v[58:59], v[48:49]\n\t"
      ".set zr%=, 104\n\t.rept 12\n\tv_mov_b64 v[zr%=:zr%=+1], 0\n\t.set zr%=, zr%=+2\n\t.endr\n\t"
      "s_barrier\n\t"
      "ds_read_b128 v[96:99], v61\n\t"
      "ds_read_b128 v[44:47], v61 offset:2048\n\t"
      VX("v[52:55]", "v61", 1024, 1280)
      "s_waitcnt lgkmcnt(3)\n\t"
      "v_mfma_f32_32x32x16_bf16 v[80:95], v[96:99], v[40:43], 0\n\t"
      "s_nop 15\n\t"
      "v_max3_f32 v64, v80, v81, v82\n\tv_max3_f32 v65, v83, v84, v85\n\tv_max3_f32 v66, v86, v87, v88\n\t"
      "v_max3_f32 v67, v89, v90, v91\n\tv_max3_f32 v68, v92, v93, v94\n\tv_max3_f32 v64, v64, v65, v95\n\t"
      "v_max3_f32 v66, v66, v67, v68\n\tv_max_f32 v64, v64, v66\n\tv_mov_b32 v65, v64\n\t"
      "s_nop 1\n\t"
      "v_permlane32_swap_b32 v64, v65\n\t"
      "s_nop 1\n\t"
      "v_min_f32_e64 v64, -v64, -v65\n\t"
      "v_mov_b32 v65, v64\n\t"
      "v_mov_b64 v[66:67], v[64:65]\n\tv_mov_b64 v[68:69], v[64:65]\n\tv_mov_b64 v[70:71], v[64:65]\n\tv_mov_b64 v[72:73], v[64:65]\n\t"
      "v_mov_b64 v[74:75], v[64:65]\n\tv_mov_b64 v[76:77], v[64:65]\n\tv_mov_b64 v[78:79], v[64:65]\n\t"
      "s_nop 1\n\t"
      "v_mfma_f32_32x32x16_bf16 v[80:95], v[96:99], v[40:43], v[64:79]\n\t"
      "s_nop 15\n\t"
      "LOOP%=:\n\t"
      T32E(P0, "v61", 4096, "v61", 3072, 3328) T32O(P1, "v61", 6144, "v61", 5120, 5376)
      T32E(P2, "v61", 8192, "v61", 7168, 7424) T32O(P3, "v61", 10240, "v61", 9216, 9472)
      T32E(P0, "v61", 12288, "v61", 11264, 11520)
      "ds_read_b32 %[c0], v61\n\t"
      T32O(P1, "v61", 14336, "v61", 13312, 13568)
      "s_barrier\n\t"
      T32E(P2, "v62", 0, "v61", 15360, 15616) T32O(P3, "v62", 2048, "v62", 1024, 1280)
      "v_mov_b32 v61, v62\n\t"
      "s_sub_u32 %[nx], %[nx], 0x4000\n\t"
      "s_cmp_lt_u32 %[nx], 0x1c000\n\t"
      "s_cselect_b32 %[nx], 0x24000, %[nx]\n\t"
      "s_sub_u32 %[cnt], %[cnt], 1\n\t"
      "v_add_u32 v62, %[nx], v63\n\t"
      "s_cmp_lg_u32 %[cnt], 0\n\t"
      "s_cbranch_scc1 LOOP%=\n\t"
      SP(3) SM("v[56:59]", "v[104:111]")
      "s_waitcnt lgkmcnt(0)\n\t"
      "s_nop 15\n\ts_nop 7\n\t"
      : [c0] "=&v"(c0), "=&{v[112:127]}"(oo), [cnt] "+s"(cnt), [nx] "+s"(nx)
      : "{v[40:43]}"(q), "{v63}"(lane), [ones] "v"(ones), [mask] "s"(VMASK)
      : "memory", "scc", CL4(44, 45, 46, 47), "v48", "v49", CL10(5), "v60", "v61", "v62", CL4(64, 65, 66, 67), "v68", "v69", CL10(7), CL10(8), CL10(9), CL10(10), "v110", "v111");
}

#define WARM 2
#define SLP 12
#define NA0 2
#define NA1 5
#define ABAR 0xAA

__global__ __launch_bounds__(1024, 4) void k_fused(const u16* __restrict__ x, const u16* __restrict__ wqkv,
                                                   const u16* __restrict__ woa, const u16* __restrict__ wq,
                                                   const u16* __restrict__ wk, const u16* __restrict__ wv,
                                                   const u16* __restrict__ wob, const u16* __restrict__ gw,
                                                   char* ws, u16* __restrict__ out) {
  __shared__ __attribute__((aligned(1024))) char smem[163840];
  const unsigned sb = (unsigned)(size_t)(LDS_AS char*)smem;
  lds_cptr S = (lds_cptr)smem;
  lds_ptr SW = (lds_ptr)smem;
  const int b = blockIdx.x, xcd = b & 7, jj = b >> 3;
  const int tb = 8 * (xcd >> 1) + (jj >> 2), hg = 4 * (xcd & 1) + (jj & 3);
  const int T0 = 64 * tb;
  const int rot = (tb + 2 * (jj & 3)) & 7;
  const int w = __builtin_amdgcn_readfirstlane(threadIdx.x >> 6);
#define LANE_VARS int l = threadIdx.x; asm volatile("" : "+v"(l)); l &= 63; \
  const int g = l >> 4, t = l & 15, q4 = t >> 2, p4 = l & 3, fsw = ((q4 >> 1) & 1) | ((g & 1) << 1)
#define WVO (((l & 7) ^ ((g & 1) << 1)) * 16)
#define RD_TAG(ep) (((u64)(unsigned)__builtin_amdgcn_readfirstlane(ep[1]) << 32) | (unsigned)__builtin_amdgcn_readfirstlane(ep[0]))

  if (w >= 8) {
    LANE_VARS;
    const int hd = w & 3, tt = (w >> 2) & 1, n = l & 31, cw = w - 8;
    __builtin_amdgcn_s_setprio(2);
    {
      const int r = 8 * cw + (l >> 3);
      const unsigned xv = (T0 + 2 * (r & 31) + (r >> 5)) * 1024 + (((l & 7) ^ ((r >> 1) & 7)) * 16);
      const unsigned wv_ = r * 1024 + (WVO ^ ((cw & 1) * 64));
#pragma unroll
      for (int i = 0; i < 4; ++i) {
        const int kc = (rot + i) & 7;
        dma2(xv, (const char*)x + kc * 128, sb + i * 8192 + cw * 1024, wv_, (const char*)wq + 128 * hg + kc * 65536, sb + L_WQ + i * 8192 + cw * 1024);
      }
    }

    const int wa = L_WQ + (8 * g + q4) * 128 + ((hd ^ fsw) * 32) + p4 * 8;
    const int xa = (32 * tt + t) * 128 + ((g ^ ((t >> 1) & 7)) * 16);
    f32x4 aq[2] = {};
    auto quarter = [&](int h) {
      bf16x8 wf[4], x0[4], x1[4];
#pragma unroll
      for (int i = 0; i < 4; ++i) {
        const int s = 4 * h + i;
        lds_cptr xp = S + (xa ^ ((s & 1) * 64)) + (s >> 1) * 8192;
        wf[i] = tr_frag(S + wa + s * 4096, 512); x0[i] = lds_b128(xp); x1[i] = lds_b128(xp + 2048);
      }
      __builtin_amdgcn_sched_barrier(0);
#pragma unroll
      for (int i = 0; i < 4; ++i) { aq[0] = MFMA16(wf[i], x0[i], aq[0]); aq[1] = MFMA16(wf[i], x1[i], aq[1]); }
    };
    WAIT_BAR(4);

    u32x2 ep = *(const LDS_AS u32x2*)(S + L_TAG);
    quarter(0);
    asm volatile("s_waitcnt vmcnt(0) lgkmcnt(0)\n\ts_barrier" : "+v"(ep) :: "memory");

    quarter(1);
    BAR();

    quarter(2);
    BAR();

    quarter(3);
    const u64 tag = RD_TAG(ep);
    const u32x4 qf = join16(pack4(aq[0] * QSCALE), pack4(aq[1] * QSCALE));
    const unsigned ones = ((l & 27) == 16) ? 0x3f803f80u : 0u;
    const u32x4 one4 = {ones, ones, ones, ones};
    f32x16 oo; unsigned c0;
    unsigned oa = WS_O + ((2 * tb + tt) * 64 + 32 + 4 * hg + hd) * 1024 + l * 16, ca = sb + L_R0 - 16384;
    const char* fa = ws + WS_FB + (tb * 8 + hg) * 256;
    asm volatile("" : "+v"(oa), "+v"(ca), "+s"(fa));
    auto fin = [&]() {
      const float f = __builtin_amdgcn_rcpf(oo[8]);
      return u32x4{pack2(oo[0] * f, oo[1] * f), pack2(oo[2] * f, oo[3] * f), pack2(oo[4] * f, oo[5] * f), pack2(oo[6] * f, oo[7] * f)};
    };

    att_loop(qf, ones, sb + l * 16, oo, c0);

    const float big = fmx(fmx(fmx(fmx(fabsf(oo[0]), fabsf(oo[1])), fabsf(oo[2])), fmx(fmx(fabsf(oo[3]), fabsf(oo[4])), fabsf(oo[5]))),
                          fmx(fmx(fabsf(oo[6]), fabsf(oo[7])), oo[8] * 134217728.f));
    u32x4 ob = fin();
    asm volatile("" : "+v"(ob));
    if (__builtin_expect(__builtin_amdgcn_ballot_w64(!(big < 1.7e38f)) != 0, 0)) {
      const bf16x8 qq = __builtin_bit_cast(bf16x8, qf);
      float m = -__builtin_inff();
#pragma unroll 1
      for (int kt = 0; kt < 64; ++kt) {
        bf16x8 kf;
        asm volatile("global_load_dwordx4 %0, %1, %2 sc1\n\ts_waitcnt vmcnt(0)" : "=&v"(kf) : "v"(WS_KV + kt * 2048 + l * 16), "s"(ws) : "memory");
        m = fmx(m, max16(MFMA32(kf, qq, f32x16{0})));
      }
      m = xmax32(m);
      oo = f32x16{0};
#pragma unroll 1
      for (int kt = 0; kt < 64; ++kt) {
        bf16x8 kf; u32x4 v0, v1;
        asm volatile("global_load_dwordx4 %0, %3, %4 sc1\n\tglobal_load_dwordx4 %1, %3, %4 offset:1024 sc1\n\t"
                     "global_load_dwordx4 %2, %3, %4 offset:1280 sc1\n\ts_waitcnt vmcnt(0)"
                     : "=&v"(kf), "=&v"(v0), "=&v"(v1) : "v"(WS_KV + kt * 2048 + l * 16), "s"(ws) : "memory");
        f32x16 s = MFMA32(kf, qq, f32x16{0});
        f32x8 p0, p1;
#pragma unroll
        for (int i = 0; i < 8; ++i) { p0[i] = __builtin_amdgcn_exp2f(s[i] - m); p1[i] = __builtin_amdgcn_exp2f(s[8 + i] - m); }
        if (n >= 16) { v0 = one4; v1 = one4; }
        oo = __builtin_amdgcn_smfmac_f32_32x32x32_bf16(__builtin_bit_cast(bf16x8, v0), __builtin_bit_cast(bf16x16, p0), oo, 0xdddd, 0, 0);
        oo = __builtin_amdgcn_smfmac_f32_32x32x32_bf16(__builtin_bit_cast(bf16x8, v1), __builtin_bit_cast(bf16x16, p1), oo, 0xdddd, 0, 0);
      }
      ob = fin();
    }
    gst16_sc1(oa, ws, ob);

    if (arrive(ca, c0) == 7) { put_flag(fa, tag); }

    return;
  }

  u64 tag = 0;
  const int tbo = 4 * xcd + (jj >> 3), cg = jj & 7;
  if (w < 4) {
    __builtin_amdgcn_s_setprio(3);
    LANE_VARS;
    u32x2 ep;
    const int tk = T0 + 16 * (hg >> 1);
    const unsigned kxo = (unsigned)((tk + t) * 1024 + 256 * w + 16 * g);
    u32x4 kx0 = gld16<0>(kxo, x), kx1 = gld16<64>(kxo, x), kx2 = gld16<128>(kxo, x), kx3 = gld16<192>(kxo, x);
    {
      const unsigned kv = (8 * ((l >> 3) & 3) + 4 * (l >> 5) + ((l >> 1) & 3)) * 32 + (l & 1) * 16;
      dma4(kv, kv + 1024, kv + 2048, kv + 3072, (const char*)((hg & 1) ? wv : wk) + 4096 * w, sb + L_R0 + 4096 * w);
    }
    const unsigned gwo = 256 * w + 16 * g;
    u32x4 g0, g1, g2, g3;
    asm volatile("s_mov_b64 exec, %6\n\tglobal_load_dwordx4 %0, %4, %5\n\tglobal_load_dwordx4 %1, %4, %5 offset:64\n\t"
                 "global_load_dwordx4 %2, %4, %5 offset:128\n\tglobal_load_dwordx4 %3, %4, %5 offset:192\n\ts_mov_b64 exec, -1"
                 : "=&v"(g0), "=&v"(g1), "=&v"(g2), "=&v"(g3) : "v"(gwo), "s"(gw), "s"(0x0001000100010001ull) : "memory");
    asm volatile("global_load_dwordx2 %0, %1, %2 sc1" : "=v"(ep) : "v"(0u), "s"(ws + WS_EPOCH) : "memory");
    const int tp = (t & 3) | ((t & 4) << 1) | ((t & 8) >> 1);
    unsigned ko = (tk >> 5) * 2048 + (g >> 1) * 512 + ((hg >> 1) & 1) * 256 + (g & 1) * 8 + ((hg & 1) ? 1024 + tp * 16 : t * 16);
    asm volatile("s_waitcnt vmcnt(0)" : "+v"(ep), "+v"(kx0), "+v"(kx1), "+v"(kx2), "+v"(kx3), "+v"(g0), "+v"(g1), "+v"(g2), "+v"(g3), "+v"(ko) :: "memory");

    f32x4 d = {0, 0, 0, 0}, dg = {0, 0, 0, 0};
    {
      lds_cptr wp = S + L_R0 + 4096 * w + l * 8;
      const bf16x8 xa[4] = {__builtin_bit_cast(bf16x8, kx0), __builtin_bit_cast(bf16x8, kx1), __builtin_bit_cast(bf16x8, kx2), __builtin_bit_cast(bf16x8, kx3)};
      const bf16x8 ga[4] = {__builtin_bit_cast(bf16x8, g0), __builtin_bit_cast(bf16x8, g1), __builtin_bit_cast(bf16x8, g2), __builtin_bit_cast(bf16x8, g3)};
      bf16x8 wf[4];
#pragma unroll
      for (int i = 0; i < 4; ++i) wf[i] = tr_frag(wp + 1024 * i, 512);
      if (hg & 1) {
#pragma unroll
        for (int i = 0; i < 4; ++i) { dg = MFMA16(ga[i], xa[i], dg); d = MFMA16(xa[i], wf[i], d); }
      } else {
#pragma unroll
        for (int i = 0; i < 4; ++i) d = MFMA16(wf[i], xa[i], d);
      }
    }
    *(LDS_AS f32x4*)(SW + L_R0 + 4096 * w + l * 16) = d;
    *(LDS_AS float*)(SW + L_R0 + 4096 * w + 1024 + l * 4) = dg[0];
    tag = RD_TAG(ep) * 6364136223846793005ull + 1442695040888963407ull;
    *(LDS_AS u64*)(SW + L_TAG + 4096 * w) = tag;

    LBAR();

    if (w == 0) {
#pragma unroll
      for (int i = 1; i < 4; ++i) d += *(const LDS_AS f32x4*)(S + L_R0 + 4096 * i + l * 16);
      const u32x2 kv = __builtin_bit_cast(u32x2, pack4(d));
      gst16_sc1(2 * ko, ws + WS_KG, u32x4{kv[0], (unsigned)tag, kv[1], (unsigned)tag});
      gst8_sc1(ko, ws + WS_KV, kv);
    } else if (w == 1 && (hg & 1)) {
      float dz = 0;
#pragma unroll
      for (int i = 0; i < 4; ++i) dz += *(const LDS_AS float*)(S + L_R0 + 4096 * i + 1024 + l * 4);
      if (l < 16) gst4_sc1(WS_G + (tk + l) * 4, ws, __float_as_uint(__builtin_amdgcn_rcpf(1.f + __expf(-dz))));
    }

    const unsigned fo = WS_FKV + (2 * w + ((l >> 2) & 1)) * 256 + p4 * 8, vo = WS_KV + w * 4096 + l * 16;
    unsigned o0 = (l >> 3) * 1024 + WVO;
    int ct = 16 * (xcd >> 1), slot = L_R0;
    {
      const unsigned go = w * 8192 + l * 16;
      const unsigned la0 = sb + w * 4096 + l * 8;
      f32x32 a;
#define GLD(R, O, P) "global_load_dwordx4 v[" STR(R) ":" STR(R) "+3], %[go], " P " offset:" STR(O) " sc1\n\t"
#define SWEEP(P) GLD(96, -4096, P) GLD(100, -3072, P) GLD(104, -2048, P) GLD(108, -1024, P) GLD(112, 0, P) GLD(116, 1024, P) GLD(120, 2048, P) GLD(124, 3072, P)
#define GCK(D, R) "v_bitop3_b32 " D ", v[" STR(R) "+1], v[" STR(R) "+3], %[tag] bitop3:0x7e\n\t"
#define GW2(A, R) "ds_write2_b32 " A ", v[" STR(R) "], v[" STR(R) "+2] offset1:1\n\tds_write2_b32 " A ", v[" STR(R) "+4], v[" STR(R) "+6] offset0:128 offset1:129\n\t"
      const char* gp = ws + WS_KG + 4096 + ct * 4096;
      LBAR();

      BAR();

      asm volatile(SWEEP("%[gp]") "s_barrier" : "={v[96:127]}"(a) : [go] "v"(go), [gp] "s"(gp) : "memory");
#pragma unroll 1
      for (int c = 0; c < 2; ++c) {
        const char* gn = gp + 32768;
        unsigned t0, t1, t2, t3, it = 0;
        asm volatile(
            "GS%=:\n\ts_waitcnt vmcnt(0)\n\t"
            GCK("%[t0]", 96) GCK("%[t1]", 100) GCK("%[t2]", 104) GCK("%[t3]", 108) "v_or3_b32 %[t0], %[t0], %[t1], %[t2]\n\t"
            GCK("%[t1]", 112) GCK("%[t2]", 116) "v_or3_b32 %[t0], %[t0], %[t3], %[t1]\n\t"
            GCK("%[t3]", 120) GCK("%[t1]", 124) "v_or3_b32 %[t0], %[t0], %[t2], %[t3]\n\tv_or_b32 %[t0], %[t0], %[t1]\n\t"
            "v_cmp_ne_u32 vcc, 0, %[t0]\n\t"
            "s_add_u32 %[it], %[it], 1\n\t"
            "s_cbranch_vccz GW%=\n\t"
            "s_cmp_gt_u32 %[it], 0x40000\n\t"
            "s_cbranch_scc1 GW%=\n\t"
            SWEEP("%[gp]")
            "s_branch GS%=\n\t"
            "GW%=:\n\t"
            "v_add_u32 %[t0], %[slot], %[la]\n\tv_add_u32 %[t1], 0x400, %[t0]\n\tv_add_u32 %[t2], 0x800, %[t0]\n\tv_add_u32 %[t3], 0xc00, %[t0]\n\t"
            GW2("%[t0]", 96) GW2("%[t1]", 104) GW2("%[t2]", 112) GW2("%[t3]", 120)
            "s_cmp_eq_u32 %[c], 1\n\t"
            "s_cbranch_scc1 GE%=\n\t"
            SWEEP("%[gn]")
            "GE%=:"
            : "+{v[96:127]}"(a), [t0] "=&v"(t0), [t1] "=&v"(t1), [t2] "=&v"(t2), [t3] "=&v"(t3), [it] "+s"(it)
            : [go] "v"(go), [gp] "s"(gp), [gn] "s"(gn), [tag] "s"((unsigned)tag), [la] "v"(la0), [slot] "s"(slot), [c] "s"(c) : "memory", "scc", "vcc");

        gp = gn; slot -= 16384;
        if (c == 1 && w == 0) put_flag(ws + WS_FKV + (tk >> 5) * 256 + (hg & 3) * 8, tag);
        LBAR();

      }
      ct = (ct + 16) & 63;
    }
#pragma unroll 1
    for (int c = 2; c < 8; ++c) {
      poll_flags(fo, ws + ct * 256, tag);

      const char* kp = ws + ct * 2048;
      u32x4 a0 = gld16_sc1<0>(vo, kp), a1 = gld16_sc1<1024>(vo, kp), a2 = gld16_sc1<2048>(vo, kp), a3 = gld16_sc1<3072>(vo, kp);
      asm volatile("s_waitcnt vmcnt(0)" : "+v"(a0), "+v"(a1), "+v"(a2), "+v"(a3) :: "memory");
      lds_ptr dp = SW + slot + w * 4096 + l * 16;
      *(LDS_AS u32x4*)dp = a0; *(LDS_AS u32x4*)(dp + 1024) = a1; *(LDS_AS u32x4*)(dp + 2048) = a2; *(LDS_AS u32x4*)(dp + 3072) = a3;
      if (c >= 6) {
        const char* p = (const char*)(c == 6 ? woa : wob) + 128 * cg + w * 32768;
        const unsigned dst = sb + (c & 1) * 65536 + w * 4096;
#pragma unroll
        for (int i = 0; i < 4; ++i)
          if (i < 3 || c == 6) dma4(o0, (o0 ^ 64) + 8192, o0 + 16384, (o0 ^ 64) + 24576, p + i * 131072, dst + i * 16384);
      }
      ct = (ct + 8) & 63; slot = slot == L_S2 ? L_R0 : slot - 16384;

      LBAR();

    }

    __builtin_amdgcn_s_setprio(0);
    if (b == 0 && w == 0) put_flag(ws + WS_EPOCH, tag);
  } else {
    LANE_VARS;
    const int hd = w & 3;
    {
      const int r = 16 * hd + (l >> 3);
      const unsigned xv = (T0 + 2 * (r & 31) + (r >> 5)) * 1024 + (((l & 7) ^ ((r >> 1) & 7)) * 16), xv1 = (xv ^ 64) + 16384;
      const unsigned wv_ = r * 1024 + WVO, wv1 = (wv_ ^ 64) + 8192;
#pragma unroll
      for (int i = 0; i < 8; ++i) {
        const int kc = (rot + 4 + (i >> 1)) & 7, lo = (4 + (i >> 1)) * 8192 + hd * 2048 + (i & 1) * 1024;
        if (i == NA0) { BAR(); }
        if (i == NA1) { u32x2 ep = *(const LDS_AS u32x2*)(S + L_TAG); tag = RD_TAG(ep); BAR(); }
        dma2((i & 1) ? xv1 : xv, (const char*)x + kc * 128, sb + lo, (i & 1) ? wv1 : wv_, (const char*)wq + 128 * hg + kc * 65536, sb + L_WQ + lo);
      }
    }
    unsigned wvo = (8 * ((l >> 3) & 3) + 4 * (l >> 5) + ((l >> 1) & 3)) * 3072 + (l & 1) * 16;
    const char* wsrc = (const char*)wqkv + (4 * hg + hd) * 32;
    const int ring = L_WQ + hd * 6144;
    const int th = T0 - 16 + 2 * (t & 7);
    unsigned hvo = ((th < 0 ? 0 : th) + (t >> 3)) * 1024 + g * 16;
    int ks = 2 * rot;
    bf16x8 hx[2] = {};
    auto stage_w = [&](int sl) {
      dma3(wvo, wsrc + ks * 98304, sb + ring + sl * 3072);
      asm volatile("global_load_dwordx4 %0, %1, %2" : "+v"(hx[sl]) : "v"(hvo), "s"((const char*)x + ks * 64) : "memory");
      ks = (ks + 1) & 15;
    };
    int xa = t * 128 + ((g ^ ((t >> 1) & 7)) * 16);
    f32x4 aa[4] = {}, ak[5] = {}, av[5] = {};
    f32x4 mo, mp, mh[2];
#pragma unroll
    for (int r = 0; r < 4; ++r) {
      mo[r] = ((unsigned)(t - 4 * g - r) <= 7u) ? 0.f : -__builtin_inff();
      mp[r] = (4 * g + r >= t + 9) ? 0.f : -__builtin_inff();
      mh[1][r] = tb > 0 ? mp[r] : -__builtin_inff();
      mh[0][r] = (tb > 0 && g < 2 && 4 * g + r > t) ? 0.f : -__builtin_inff();
    }
    int wr = ring + l * 8;
    asm volatile("" : "+v"(aa[0]), "+v"(aa[1]), "+v"(aa[2]), "+v"(aa[3]), "+v"(ak[0]), "+v"(ak[1]), "+v"(ak[2]), "+v"(ak[3]), "+v"(ak[4]),
                      "+v"(av[0]), "+v"(av[1]), "+v"(av[2]), "+v"(av[3]), "+v"(av[4]), "+v"(mo), "+v"(mp), "+v"(mh[0]), "+v"(mh[1]),
                      "+v"(hx[0]), "+v"(hx[1]), "+v"(xa), "+v"(wr), "+v"(wvo), "+v"(hvo));

    WAIT_BAR(8);
    WAIT_BAR(0);

    BAR();
    stage_w(0); stage_w(1);
    BAR();
#pragma unroll 1
    for (int j = 0; j < 8; ++j) {
#pragma unroll
      for (int h = 0; h < 2; ++h) {
        bf16x8 wf[3], xf[4];
#pragma unroll
        for (int i = 0; i < 4; ++i) xf[i] = lds_b128(S + (xa ^ (h * 64)) + i * 2048);
        asm volatile("s_waitcnt vmcnt(4)" : "+v"(hx[h]) :: "memory");
#pragma unroll
        for (int m = 0; m < 3; ++m) wf[m] = tr_frag(S + wr + h * 3072 + m * 1024, 512);
        asm volatile("s_waitcnt lgkmcnt(0)" : "+v"(wf[0]), "+v"(wf[1]), "+v"(wf[2]) :: "memory");
        ak[4] = MFMA16(wf[1], hx[h], ak[4]); av[4] = MFMA16(hx[h], wf[2], av[4]);
        asm volatile("" : "+v"(ak[4]), "+v"(av[4]));
        stage_w(h);
#pragma unroll
        for (int i = 0; i < 4; ++i) {
          aa[i] = MFMA16(wf[0], xf[i], aa[i]);
          ak[i] = MFMA16(wf[1], xf[i], ak[i]); av[i] = MFMA16(xf[i], wf[2], av[i]);
        }
      }
      xa += 8192;

      if ((ABAR >> j) & 1) { BAR(); }
    }
    asm volatile("s_waitcnt vmcnt(0)" : "+v"(hx[0]), "+v"(hx[1]) :: "memory");

    const s16x4 kh_ = pack4(ak[4]), vh_ = pack4(av[4]);
#pragma unroll
    for (int tt = 0; tt < 2; ++tt) {
      s16x4 kk[3] = {kh_, pack4(ak[2 * tt]), pack4(ak[2 * tt + 1])}, vv[3] = {vh_, pack4(av[2 * tt]), pack4(av[2 * tt + 1])};
      const s16x4 on = {0x3f80, 0x3f80, 0x3f80, 0x3f80};
      s16x4 ob[2], po[2], pp[2];
      f32x4 so[2], sp[2], o[2], ls[2];
      float m[2];
      const f32x4 z = {0, 0, 0, 0};
#pragma unroll
      for (int i = 0; i < 2; ++i) {
        s16x4 q = pack4(aa[2 * tt + i] * QSCALE);
        so[i] = MFMA16K(kk[i + 1], q, mo); sp[i] = MFMA16K(kk[i], q, i ? mp : mh[tt]);
      }
#pragma unroll
      for (int i = 0; i < 2; ++i) m[i] = fmx(fmx(fmx(fmx(so[i][0], so[i][1]), so[i][2]), fmx(fmx(so[i][3], sp[i][0]), sp[i][1])), fmx(sp[i][2], sp[i][3]));
      {
        auto r = __builtin_amdgcn_permlane16_swap(__float_as_uint(m[0]), __float_as_uint(m[1]), false, false);
        float y = xmax32(fmx(__uint_as_float(r[0]), __uint_as_float(r[1])));
        auto u = __builtin_amdgcn_permlane16_swap(__float_as_uint(y), __float_as_uint(y), false, false);
        m[0] = __uint_as_float(u[0]); m[1] = __uint_as_float(u[1]);
      }
#pragma unroll
      for (int i = 0; i < 2; ++i) {
#pragma unroll
        for (int r = 0; r < 4; ++r) { so[i][r] = __builtin_amdgcn_exp2f(so[i][r] - m[i]); sp[i][r] = __builtin_amdgcn_exp2f(sp[i][r] - m[i]); }
        po[i] = pack4(so[i]); pp[i] = pack4(sp[i]);
        ls[i] = MFMA16K(on, pp[i], MFMA16K(on, po[i], z));
        o[i] = MFMA16K(vv[i], pp[i], MFMA16K(vv[i + 1], po[i], z));
      }
#pragma unroll
      for (int i = 0; i < 2; ++i) ob[i] = pack4(o[i] * __builtin_amdgcn_rcpf(ls[i][0]));
      gst16_sc1(WS_O + ((2 * tb + tt) * 64 + 4 * hg + hd) * 1024 + l * 16, ws, join16(ob[0], ob[1]));

    }
    unsigned c0 = *(const LDS_AS unsigned*)(S + L_S2);
    asm volatile("s_waitcnt lgkmcnt(0)\n\ts_barrier" : "+v"(c0) :: "memory");

    if (arrive(sb + L_S2, c0) == 3) put_flag(ws + WS_FA + (tb * 8 + hg) * 256, tag);

    asm volatile("global_store_dwordx4 %0, %1, %2\n\tglobal_store_dwordx4 %0, %1, %2 offset:64\n\ts_nop 1" :: "v"((64 * tbo + 16 * hd + (l >> 2)) * 1024 + 128 * cg + (l & 3) * 16), "v"(mo), "s"(out) : "memory");
    unsigned o0 = (l >> 3) * 1024 + WVO, o1 = (o0 ^ 64) + 8192, o2 = o0 + 16384, o3 = o1 + 16384;
    asm volatile("s_barrier" : "+v"(o0), "+v"(o1), "+v"(o2), "+v"(o3) :: "memory");

    dma4(o0, o1, o2, o3, (const char*)wob + 128 * cg + hd * 32768 + 393216, sb + L_S2 + hd * 4096);

  }

  LANE_VARS;
  const int to = (w >> 1) & 1, hf = w & 1, kh = w >> 2;
  const unsigned ovo = WS_O + ((2 * tbo + to) * 64 + 16 * kh + (g >> 1)) * 1024 + ((g & 1) * 32 + 16 * hf + t) * 16;
  const int tok = 64 * tbo + 2 * (16 * hf + t) + to;
  const unsigned oso = tok * 1024 + 128 * cg + 64 * kh + 32 * (g & 1) + 16 * (g >> 1);
  int wo = ((8 * g + q4) * 128 + fsw * 32 + p4 * 8 + kh * 32768) ^ (64 * kh);
  const int rb = L_R0 + w * 2048 + l * 16;
  unsigned fo = tbo * 2048 + (l & 7) * 256;
  f32x4 keep[4] = {};
  const char* fl = ws + WS_FA;
  const char* ob = ws;
#pragma unroll 1
  for (int pass = 0;; ++pass) {
    asm volatile("" : "+s"(pass));
    bf16x8 wf[4][4];
#pragma unroll
    for (int j = 0; j < 4; ++j)
#pragma unroll
      for (int c = 0; c < 4; ++c) wf[j][c] = tr_frag(S + (wo ^ (32 * c)) + j * 4096, 512);
    poll_flags(fo, fl, tag);

    u32x4 of[8];
    float gs;
    of[0] = gld16_sc1<0>(ovo, ob); of[1] = gld16_sc1<2048>(ovo, ob); of[2] = gld16_sc1<0>(ovo, ob + 4096); of[3] = gld16_sc1<2048>(ovo, ob + 4096);
    of[4] = gld16_sc1<0>(ovo, ob + 8192); of[5] = gld16_sc1<2048>(ovo, ob + 8192); of[6] = gld16_sc1<0>(ovo, ob + 12288); of[7] = gld16_sc1<2048>(ovo, ob + 12288);
    asm volatile("global_load_dword %0, %1, %2 sc1" : "=v"(gs) : "v"(WS_G + tok * 4), "s"(ws) : "memory");
    f32x4 acc[4] = {};
    if (!pass) { BAR(); __builtin_amdgcn_s_sleep(SLP); }
    asm volatile("s_waitcnt vmcnt(5)" : "+v"(of[0]), "+v"(of[1]), "+v"(of[2]), "+v"(of[3]) :: "memory");

#pragma unroll
    for (int j = 0; j < 4; ++j)
#pragma unroll
      for (int c = 0; c < 4; ++c) {
        acc[c] = MFMA16(wf[j][c], __builtin_bit_cast(bf16x8, of[j]), acc[c]);
        wf[j][c] = tr_frag(S + (wo ^ (32 * c)) + (4 + j) * 4096, 512);
      }
    asm volatile("s_waitcnt vmcnt(0)" : "+v"(of[4]), "+v"(of[5]), "+v"(of[6]), "+v"(of[7]), "+v"(gs) :: "memory");
#pragma unroll
    for (int j = 0; j < 4; ++j)
#pragma unroll
      for (int c = 0; c < 4; ++c) acc[c] = MFMA16(wf[j][c], __builtin_bit_cast(bf16x8, of[4 + j]), acc[c]);
    asm volatile("s_nop 7\n\ts_nop 1" : "+v"(acc[0]), "+v"(acc[1]), "+v"(acc[2]), "+v"(acc[3]));

    if (pass) gs = 1.f - gs;
#pragma unroll
    for (int c = 3; c >= 0; --c) keep[c] += acc[c] * gs;
    if (pass) break;

    fl += WS_FB - WS_FA; ob += 32768; wo += 65536; fo = (fo & ~0x400u) | (kh << 10);
  }
  *(LDS_AS f32x4*)(SW + rb) = keep[2];
  *(LDS_AS f32x4*)(SW + rb + 1024) = keep[3];
  LBAR();
  lds_cptr pr = S + (rb ^ 8192);
  const f32x4 r0 = keep[0] + *(const LDS_AS f32x4*)(pr), r1 = keep[1] + *(const LDS_AS f32x4*)(pr + 1024);
  *(u32x4*)((char*)out + oso) = join16(pack4(r0), pack4(r1));

}

extern "C" void kernel_launch(void* const* d_in, const int* in_sizes, int n_in, void* d_out, int out_size, void* d_ws,
                              size_t ws_size, hipStream_t stream) {
  k_fused<<<256, 1024, 0, stream>>>((const u16*)d_in[0], (const u16*)d_in[1], (const u16*)d_in[2], (const u16*)d_in[4],
                                    (const u16*)d_in[5], (const u16*)d_in[6], (const u16*)d_in[7], (const u16*)d_in[8],
                                    (char*)d_ws, (u16*)d_out);
}
```

```cpp
#include <hip/hip_runtime.h>
#include <hip/hip_bf16.h>
#include <type_traits>

typedef unsigned short u16;
typedef unsigned long long u64;
typedef __attribute__((ext_vector_type(2))) float f32x2;
typedef __attribute__((ext_vector_type(2))) __bf16 bf16x2;
typedef __attribute__((ext_vector_type(4))) float f32x4;
typedef __attribute__((ext_vector_type(8))) float f32x8;
typedef __attribute__((ext_vector_type(16))) float f32x16;
typedef __attribute__((ext_vector_type(32))) float f32x32;
typedef __attribute__((ext_vector_type(16))) __bf16 bf16x16;
typedef __attribute__((ext_vector_type(8))) __bf16 bf16x8;
typedef __attribute__((ext_vector_type(4))) __bf16 bf16x4;
typedef __attribute__((ext_vector_type(4))) short s16x4;
typedef __attribute__((ext_vector_type(8))) short s16x8;
typedef __attribute__((ext_vector_type(4))) unsigned u32x4;
typedef __attribute__((ext_vector_type(2))) unsigned u32x2;
typedef __attribute__((ext_vector_type(8))) unsigned u32x8;
typedef __attribute__((ext_vector_type(16))) unsigned u32x16;
#define LDS_AS __attribute__((address_space(3)))
typedef LDS_AS const char* lds_cptr;
typedef LDS_AS char* lds_ptr;

#define MFMA32(a, b, c) __builtin_amdgcn_mfma_f32_32x32x16_bf16(a, b, c, 0, 0, 0)
#define MFMA16(a, b, c) __builtin_amdgcn_mfma_f32_16x16x32_bf16(a, b, c, 0, 0, 0)
#define MFMA16K(a, b, c) __builtin_amdgcn_mfma_f32_16x16x16bf16_1k(a, b, c, 0, 0, 0)
#define WAIT_BAR(N) asm volatile("s_waitcnt vmcnt(" #N ") lgkmcnt(0)\n\ts_barrier" ::: "memory")
#define LBAR() asm volatile("s_waitcnt lgkmcnt(0)\n\ts_barrier" ::: "memory")
#define BAR() asm volatile("s_barrier" ::: "memory")
#define STR2(x) #x
#define STR(x) STR2(x)

constexpr unsigned WS_EPOCH = 0;
constexpr unsigned WS_FKV = 4096;
constexpr unsigned WS_G = 32768;
constexpr unsigned WS_KV = 65536;
constexpr unsigned WS_FA = 262144;
constexpr unsigned WS_FB = WS_FA + 128;
#define FSP 256
constexpr unsigned WS_KG = 524288;
constexpr unsigned WS_O = 1u << 20;

constexpr float QSCALE = 0.25f * 1.4426950408889634f;

__device__ __forceinline__ void dma4(unsigned v0, unsigned v1, unsigned v2, unsigned v3, const void* sbase, unsigned lds) {
  unsigned sv;
  asm volatile("s_mov_b32 %0, m0\n\ts_mov_b32 m0, %6\n\ts_nop 0\n\tglobal_load_lds_dwordx4 %1, %5\n\t"
               "s_add_u32 m0, m0, 0x400\n\ts_nop 0\n\tglobal_load_lds_dwordx4 %2, %5\n\t"
               "s_add_u32 m0, m0, 0x400\n\ts_nop 0\n\tglobal_load_lds_dwordx4 %3, %5\n\t"
               "s_add_u32 m0, m0, 0x400\n\ts_nop 0\n\tglobal_load_lds_dwordx4 %4, %5\n\ts_mov_b32 m0, %0"
               : "=&s"(sv) : "v"(v0), "v"(v1), "v"(v2), "v"(v3), "s"(sbase), "s"(lds) : "memory", "scc");
}
__device__ __forceinline__ void dma3(unsigned v0, const void* s0, unsigned l0) {
  unsigned sv;
  asm volatile("s_mov_b32 %0, m0\n\ts_mov_b32 m0, %3\n\ts_nop 0\n\tglobal_load_lds_dwordx4 %1, %2\n\t"
               "global_load_lds_dwordx4 %1, %2 offset:1024\n\tglobal_load_lds_dwordx4 %1, %2 offset:2048\n\ts_mov_b32 m0, %0"
               : "=&s"(sv) : "v"(v0), "s"(s0), "s"(l0) : "memory");
}
template <int OFF> __device__ __forceinline__ u32x4 gld16_sc1(unsigned voff, const void* sbase) {
  u32x4 r;
  asm volatile("global_load_dwordx4 %0, %1, %2 offset:%3 sc1" : "=v"(r) : "v"(voff), "s"(sbase), "n"(OFF) : "memory");
  return r;
}
__device__ __forceinline__ void gst16_sc1(unsigned voff, void* sbase, u32x4 v) {
  asm volatile("global_store_dwordx4 %0, %1, %2 sc1\n\ts_nop 1" :: "v"(voff), "v"(v), "s"(sbase) : "memory");
}
__device__ __forceinline__ void gst8_sc1(unsigned voff, void* sbase, u32x2 v) {
  asm volatile("global_store_dwordx2 %0, %1, %2 sc1\n\ts_nop 1" :: "v"(voff), "v"(v), "s"(sbase) : "memory");
}
__device__ __forceinline__ void gst4_sc1(unsigned voff, void* sbase, unsigned v) {
  asm volatile("global_store_dword %0, %1, %2 sc1\n\ts_nop 1" :: "v"(voff), "v"(v), "s"(sbase) : "memory");
}
__device__ __forceinline__ void put_flag(const void* p, u64 tag) {
  unsigned a; u64 v;
  asm volatile("s_mov_b64 exec, 1\n\tv_mov_b32 %0, 0\n\tv_mov_b64 %1, %2\n\tglobal_store_dwordx2 %0, %1, %3 sc1\n\ts_mov_b64 exec, -1\n\ts_nop 1"
               : "=&v"(a), "=&v"(v) : "s"(tag), "s"(p) : "memory");
}
__device__ __forceinline__ unsigned arrive(unsigned lds, unsigned c0) {
  unsigned old;
  asm volatile("s_waitcnt vmcnt(0)\n\ts_mov_b64 exec, 1\n\tds_add_rtn_u32 %0, %1, %2\n\ts_waitcnt lgkmcnt(0)\n\ts_mov_b64 exec, -1"
               : "=&v"(old) : "v"(lds), "v"(1u) : "memory");
  return __builtin_amdgcn_readfirstlane(old - c0);
}
__device__ __forceinline__ void poll_flags(unsigned voff, const void* sbase, u64 tag) {
#pragma unroll 1
  for (int it = 0; it < (1 << 20); ++it) {
    u64 f;
    asm volatile("global_load_dwordx2 %0, %1, %2 sc1\n\ts_waitcnt vmcnt(0)" : "=&v"(f) : "v"(voff), "s"(sbase) : "memory");
    if (__builtin_amdgcn_ballot_w64(f != tag) == 0) break;
    __builtin_amdgcn_s_sleep(2);
  }
}
__device__ __forceinline__ bf16x8 lds_b128(lds_cptr p) { return *(const LDS_AS bf16x8*)p; }
__device__ __forceinline__ s16x4 lds_tr(lds_cptr p) {
  return __builtin_amdgcn_ds_read_tr16_b64_v4i16((LDS_AS s16x4*)p);
}
__device__ __forceinline__ bf16x8 tr_frag(lds_cptr p, int off2) {
  s16x4 a = lds_tr(p), b = lds_tr(p + off2);
  s16x8 r = __builtin_shufflevector(a, b, 0, 1, 2, 3, 4, 5, 6, 7);
  return __builtin_bit_cast(bf16x8, r);
}
__device__ __forceinline__ unsigned pack2(float a, float b) {
  return __builtin_bit_cast(unsigned, __builtin_convertvector(f32x2{a, b}, bf16x2));
}
__device__ __forceinline__ s16x4 pack4(f32x4 a) {
  u32x2 r = {pack2(a[0], a[1]), pack2(a[2], a[3])};
  asm("" : "+v"(r));
  return __builtin_bit_cast(s16x4, r);
}
__device__ __forceinline__ float fmx(float a, float b) { return __builtin_elementwise_maximum(a, b); }
__device__ __forceinline__ u32x4 join16(s16x4 a, s16x4 b) {
  u32x2 x = __builtin_bit_cast(u32x2, a), y = __builtin_bit_cast(u32x2, b);
  auto r0 = __builtin_amdgcn_permlane16_swap(x[0], y[0], false, false);
  auto r1 = __builtin_amdgcn_permlane16_swap(x[1], y[1], false, false);
  return u32x4{r0[0], r1[0], r0[1], r1[1]};
}
__device__ __forceinline__ float xmax32(float v) {
  auto r = __builtin_amdgcn_permlane32_swap(__float_as_uint(v), __float_as_uint(v), false, false);
  return fmx(__uint_as_float(r[0]), __uint_as_float(r[1]));
}
__device__ __forceinline__ float xmax16(float v) {
  auto r = __builtin_amdgcn_permlane16_swap(__float_as_uint(v), __float_as_uint(v), false, false);
  return fmx(__uint_as_float(r[0]), __uint_as_float(r[1]));
}
__device__ __forceinline__ float max16(const f32x16& s) {
  float a = fmx(fmx(s[0], s[1]), s[2]);
#pragma unroll
  for (int i = 3; i < 15; i += 2) a = fmx(fmx(a, s[i]), s[i + 1]);
  return fmx(a, s[15]);
}

constexpr int L_WQ = 65536, L_R0 = 147456;
constexpr int L_TAG = L_R0 + 2048;
constexpr int L_GW = 131072, L_EP = L_GW + 1024, L_PF = L_EP + 256;
constexpr int L_QX = 90112;
constexpr int L_GT = 98304;
constexpr int L_S2 = 114688;

#define EX(r) "v_exp_f32 v" STR(r) ", v" STR(r) "\n\t"
#define E3(a, b, c) EX(a) EX(b) EX(c)
#define E5(a, b, c, d, e) EX(a) EX(b) EX(c) EX(d) EX(e)
#define SM(O, V, S) "v_smfmac_f32_32x32x32_bf16 " O ", " V ", " S ", v60\n\t"
#define LW(N) "s_waitcnt lgkmcnt(" #N ")\n\t"
#define MF(S, Q, C) "v_mfma_f32_32x32x16_bf16 " S ", v[44:47], " Q ", " C "\n\t"
#define VX(V1, VA, O0) "s_mov_b64 exec, %[mask]\n\tds_read_b128 v[48:51], " VA " offset:" STR(O0) "+1024\n\tds_read_b128 " V1 ", " VA " offset:" STR(O0) "+1280\n\ts_mov_b64 exec, -1\n\t"
#define SP(p) "s_setprio " STR(p) "\n\t"
#define T32E(P, VP, NA, NO) EX(80) EX(81) SP(P) SM("v[112:127]", VP, "v[104:111]") EX(82) EX(83) EX(84) EX(85) EX(86) MF("v[96:111]", "v[40:43]", "v[64:79]") \
  "ds_read_b128 v[44:47], " NA " offset:" STR(NO) "\n\t" EX(87) EX(88) EX(89) EX(90) EX(91) LW(1) SM("v[4:19]", "v[48:51]", "v[80:87]") EX(92) EX(93) EX(94) EX(95)
#define T32O(P, VC, VP, NA, NO, X) EX(96) EX(97) SP(P) SM("v[4:19]", VC, "v[88:95]") EX(98) EX(99) EX(100) EX(101) EX(102) X LW(0) MF("v[80:95]", "v[36:39]", "v[20:35]") \
  EX(103) EX(104) EX(105) EX(106) EX(107) SM("v[112:127]", "v[48:51]", "v[96:103]") VX(VP, NA, NO) EX(108) EX(109) EX(110) EX(111)
#define RV(C, i) "v[" STR(C) "+" STR(i) "]"
#define MX3(C, d, a, b) "v_max3_f32 " RV(C, d) ", " RV(C, d) ", " RV(C, a) ", " RV(C, b) "\n\t"
#define MXA(C) MX3(C, 0, 1, 2) MX3(C, 3, 4, 5) MX3(C, 6, 7, 8) MX3(C, 9, 10, 11) MX3(C, 12, 13, 14) MX3(C, 0, 3, 15) MX3(C, 6, 9, 12) \
  "v_max_f32 " RV(C, 0) ", " RV(C, 0) ", " RV(C, 6) "\n\tv_mov_b32 " RV(C, 1) ", " RV(C, 0) "\n\t"
#define MXB(C) "v_min_f32_e64 " RV(C, 0) ", -" RV(C, 0) ", -" RV(C, 1) "\n\tv_mov_b32 " RV(C, 1) ", " RV(C, 0) "\n\t" \
  ".set cr%=, " STR(C) "+2\n\t.rept 7\n\tv_mov_b64 v[cr%=:cr%=+1], v[" STR(C) ":" STR(C) "+1]\n\t.set cr%=, cr%=+2\n\t.endr\n\t"
constexpr u64 VMASK = 0x0000FFFF0000FFFFull;
#define P0 3
#define P1 2
#define P2 1
#define P3 0

#define CL4(a, b, c, d) "v" #a, "v" #b, "v" #c, "v" #d
#define CL10(a) "v" STR(a##0), "v" STR(a##1), "v" STR(a##2), "v" STR(a##3), "v" STR(a##4), "v" STR(a##5), "v" STR(a##6), "v" STR(a##7), "v" STR(a##8), "v" STR(a##9)
struct AttRegs { f32x16 oo, o0; f32x8 s1; u32x8 va; u32x4 vb; u32x4 ad; };
__device__ __forceinline__ void att_init(AttRegs& r, unsigned ones, unsigned lane) {
  asm volatile(
      "v_mov_b32 v48, %[ones]\n\tv_mov_b32 v49, %[ones]\n\tv_mov_b32 v60, 0xdddd\n\tv_mov_b32 v63, %[lane]\n\t"
      "v_add_u32 v61, 0x24000, v63\n\tv_add_u32 v62, 0x20000, v63\n\t"
      "v_mov_b64 v[50:51], v[48:49]\n\tv_mov_b64 v[52:53], v[48:49]\n\tv_mov_b64 v[54:55], v[48:49]\n\tv_mov_b64 v[56:57], v[48:49]\n\tv_mov_b64 v[58:59], v[48:49]\n\t"
      ".set zr%=, 104\n\t.rept 12\n\tv_mov_b64 v[zr%=:zr%=+1], 0\n\t.set zr%=, zr%=+2\n\t.endr\n\t"
      ".set zr%=, 4\n\t.rept 8\n\tv_mov_b64 v[zr%=:zr%=+1], 0\n\t.set zr%=, zr%=+2\n\t.endr\n\t"
      : "=&{v[112:127]}"(r.oo), "=&{v[4:19]}"(r.o0), "=&{v[104:111]}"(r.s1), "=&{v[48:55]}"(r.va), "=&{v[56:59]}"(r.vb), "=&{v[60:63]}"(r.ad)
      : [ones] "v"(ones), [lane] "v"(lane));
}
__device__ __forceinline__ void att_loop(u32x4& q, unsigned qo, unsigned xs, unsigned xr, AttRegs& r, float& gate, unsigned ga) {
  unsigned cnt = 7, nx = L_R0 - 16384;
  asm volatile(
      "v_add_u32 v80, %[qo], v63\n\tv_and_b32 v82, 0xfffff7ff, v61\n\tv_xor_b32 v81, 0x1000, v80\n\t"
      "s_waitcnt lgkmcnt(0)\n\ts_barrier\n\t"
      "ds_read_b128 v[36:39], v81\n\t"
      "ds_read_b128 v[96:99], v82\n\t"
      "ds_read_b128 v[40:43], v80\n\t"
      "ds_read_b128 v[44:47], v61\n\t"
      VX("v[52:55]", "v61", 0)
      "s_waitcnt lgkmcnt(4)\n\t"
      "v_mfma_f32_32x32x16_bf16 v[20:35], v[96:99], v[36:39], 0\n\t"
      "s_waitcnt lgkmcnt(3)\n\t"
      "v_mfma_f32_32x32x16_bf16 v[64:79], v[96:99], v[40:43], 0\n\t"
      "s_nop 12\n\t"
      MXA(20)
      "s_nop 1\n\t"
      "v_permlane32_swap_b32 v20, v21\n\t"
      "s_nop 1\n\t"
      MXB(20)
      "s_nop 0\n\t" LW(2)
      MF("v[80:95]", "v[36:39]", "v[20:35]")
      MXA(64)
      "s_nop 1\n\t"
      "v_permlane32_swap_b32 v64, v65\n\t"
      "s_nop 1\n\t"
      MXB(64)
      "LOOP%=:\n\t"
      T32E(P0, "v[56:59]", "v61", 4096) T32O(P1, "v[52:55]", "v[56:59]", "v61", 4096, "")
      T32E(P2, "v[52:55]", "v61", 8192) T32O(P3, "v[56:59]", "v[52:55]", "v61", 8192, "")
      T32E(P0, "v[56:59]", "v61", 12288)
      "s_cmp_eq_u32 %[cnt], 3\n\t"
      "s_cselect_b64 exec, -1, 0\n\t"
      "ds_read_b32 %[gate], %[ga]\n\t"
      "s_mov_b64 exec, -1\n\t"
      T32O(P1, "v[52:55]", "v[56:59]", "v61", 12288, "")
      "s_barrier\n\t"
      T32E(P2, "v[52:55]", "v62", 0) T32O(P3, "v[56:59]", "v[52:55]", "v62", 0, "s_sub_u32 %[cnt], %[cnt], 1\n\ts_cbranch_scc1 TAIL%=\n\t")
      "v_mov_b32 v61, v62\n\t"
      "s_sub_u32 %[nx], %[nx], 0x4000\n\t"
      "s_cmp_lt_u32 %[nx], 0x1c000\n\t"
      "s_cselect_b32 %[nx], 0x24000, %[nx]\n\t"
      "v_add_u32 v62, %[nx], v63\n\t"
      "s_branch LOOP%=\n\t"
      "TAIL%=:\n\t"
      SP(3) "v_add_u32 v61, %[xs], v63\n\tv_add_u32 v62, %[xr], v63\n\tv_sub_f32 %[gate], 1.0, %[gate]\n\t"
      EX(103) EX(104) EX(105)
      "ds_write_b128 v61, v[4:7]\n\tds_write_b128 v61, v[8:11] offset:1024\n\tds_write_b32 v61, v12 offset:2048\n\t"
      SM("v[112:127]", "v[48:51]", "v[96:103]")
      E3(106, 107, 108) E3(109, 110, 111) "s_nop 1\n\t" SM("v[112:127]", "v[56:59]", "v[104:111]")
      "s_waitcnt lgkmcnt(0)\n\ts_barrier\n\t"
      "ds_read_b32 v88, v62 offset:2048\n\tds_read_b128 v[80:83], v62\n\tds_read_b128 v[84:87], v62 offset:1024\n\t"
      "s_nop 6\n\t"
      "s_waitcnt lgkmcnt(0)\n\tv_add_f32 v121, v120, v88\n\tv_add_f32 v120, v120, v88\n\t"
      "v_pk_add_f32 v[112:113], v[112:113], v[80:81]\n\tv_pk_add_f32 v[114:115], v[114:115], v[82:83]\n\t"
      "v_permlane32_swap_b32 v120, v121\n\t"
      "v_pk_add_f32 v[116:117], v[116:117], v[84:85]\n\tv_pk_add_f32 v[118:119], v[118:119], v[86:87]\n\t"
      : [gate] "=&v"(gate), "+{v[112:127]}"(r.oo), "+{v[4:19]}"(r.o0), "+{v[104:111]}"(r.s1), "+{v[48:55]}"(r.va), "+{v[56:59]}"(r.vb),
        "+{v[60:63]}"(r.ad), [cnt] "+s"(cnt), [nx] "+s"(nx), "=&{v[40:43]}"(q)
      : [qo] "s"(qo), [xs] "s"(xs), [xr] "s"(xr), [mask] "s"(VMASK), [ga] "v"(ga)
      : "memory", "scc", CL10(2), CL10(3), CL4(44, 45, 46, 47), CL4(64, 65, 66, 67), "v68", "v69", CL10(7), CL10(8), CL10(9), CL4(100, 101, 102, 103));
}

#define SLP 12
#define OWF(F, A, O) "ds_read_b64_tr_b16 v[" STR(F) ":" STR(F) "+1], " A " offset:" STR(O) "\n\tds_read_b64_tr_b16 v[" STR(F) "+2:" STR(F) "+3], " A " offset:" STR(O) "+512\n\t"
#define OW4(F, O) OWF(F, "%[w0]", O) OWF(F+4, "%[w1]", O) OWF(F+8, "%[w2]", O) OWF(F+12, "%[w3]", O)
#define OACC(C) "v[" STR(C) ":" STR(C) "+3]"
#define OMF(C, F, D, Z) "v_mfma_f32_16x16x32_bf16 v[" STR(C) ":" STR(C) "+3], v[" STR(F) ":" STR(F) "+3], v[" STR(D) ":" STR(D) "+3], " Z "\n\t"
#define OM4Z(F, D, O) OMF(16, F, D, "0") OWF(F, "%[w0]", O) OMF(20, F+4, D, "0") OWF(F+4, "%[w1]", O) OMF(24, F+8, D, "0") OWF(F+8, "%[w2]", O) OMF(28, F+12, D, "0") OWF(F+12, "%[w3]", O)
#define OM4R(F, D, O) OMF(16, F, D, OACC(16)) OWF(F, "%[w0]", O) OMF(20, F+4, D, OACC(20)) OWF(F+4, "%[w1]", O) \
  OMF(24, F+8, D, OACC(24)) OWF(F+8, "%[w2]", O) OMF(28, F+12, D, OACC(28)) OWF(F+12, "%[w3]", O)
#define OM4(F, D) OMF(16, F, D, OACC(16)) OMF(20, F+4, D, OACC(20)) OMF(24, F+8, D, OACC(24)) OMF(28, F+12, D, OACC(28))
#define OLW(N) "s_waitcnt lgkmcnt(" #N ")\n\t"
#define OVW(N) "s_waitcnt vmcnt(" #N ")\n\t"
#define OLD(D, S) "global_load_dwordx4 v[" STR(D) ":" STR(D) "+3], %[vo], " S " offset:-4096 sc1\n\tglobal_load_dwordx4 v[" STR(D) "+4:" STR(D) "+7], %[vo], " S " offset:-2048 sc1\n\t" \
  "global_load_dwordx4 v[" STR(D) "+8:" STR(D) "+11], %[vo], " S " sc1\n\tglobal_load_dwordx4 v[" STR(D) "+12:" STR(D) "+15], %[vo], " S " offset:2048 sc1\n\t"
#define OPL(R) "global_load_dwordx2 v[" STR(R) ":" STR(R) "+1], %[fo], %[fb] sc1\n\t"
#define PD 1
#define PSL 2
#define OPC(R) "s_waitcnt vmcnt(" STR(PD) "-1)\n\tv_cmp_ne_u64 vcc, %[tag], v[" STR(R) ":" STR(R) "+1]\n\ts_cbranch_vccz OB%=\n\ts_sleep " STR(PSL) "\n\t" OPL(R)
#define OXR "ds_read_b128 v[32:35], %[xa]\n\tds_read_b128 v[36:39], %[xa] offset:1024\n\tds_read_b128 v[40:43], %[xa] offset:2048\n\tds_read_b128 v[44:47], %[xa] offset:3072\n\t"
#define OAD(A) "v_pk_add_f32 v[" STR(A) ":" STR(A) "+1], v[" STR(A) ":" STR(A) "+1], v[" STR(A) "+16:" STR(A) "+17]\n\t"
#define OCV(D, A) "v_cvt_pk_bf16_f32 v" STR(D) ", v[" STR(A) "], v[" STR(A) "+1]\n\t"
__device__ __forceinline__ void out_proj(unsigned vo, unsigned fo, unsigned w0, unsigned lv, unsigned oso, unsigned xb, unsigned cb,
                                         u64 tag, const char* ab, const char* fb, u16* out
                                         ) {
  unsigned gt, spin = 0, w1, w2, w3, ctl;
  asm volatile(
      OLD(96, "%[a0]") OLD(112, "%[a1]")
      "v_xor_b32 %[w1], 32, %[w0]\n\tv_xor_b32 %[w2], 64, %[w0]\n\tv_xor_b32 %[w3], 0x60, %[w0]\n\t"
      OW4(32, 0) OW4(48, 4096) OW4(64, 8192) OW4(80, 12288)
      "s_barrier\n\t"
      "s_waitcnt lgkmcnt(0)\n\ts_barrier\n\t"
      "s_sleep " STR(SLP) "\n\t"
      "s_waitcnt vmcnt(0) lgkmcnt(0)\n\t"
      OM4Z(32, 96, 16384) OM4R(48, 100, 20480) OM4R(64, 104, 24576) OM4R(80, 108, 28672)
      "v_add_u32 %[w0], 0x10000, %[w0]\n\tv_add_u32 %[w1], 0x10000, %[w1]\n\tv_add_u32 %[w2], 0x10000, %[w2]\n\tv_add_u32 %[w3], 0x10000, %[w3]\n\t"
      "v_add_u32 %[xa], %[xb], %[xa]\n\tv_mov_b32 %[ctl], %[cb]\n\t"
      OPL(10)
      OLW(15) OM4R(32, 112, 0) OLW(15) OM4R(48, 116, 4096)
      OLW(15) OM4R(64, 120, 8192) OLW(15) OM4R(80, 124, 12288)
      "OP%=:\n\t"
      OPC(10)
      "s_add_u32 %[spin], %[spin], 1\n\t"
      "s_cmp_lt_u32 %[spin], 0x40000\n\t"
      "s_cbranch_scc1 OP%=\n\t"
      "OB%=:\n\t"
      OLD(96, "%[c0]") OLD(112, "%[c1]")
      "v_mov_b32 %[fo], 1\n\t"
      "s_waitcnt vmcnt(7) lgkmcnt(0)\n\t"
      OM4R(32, 96, 16384) OVW(6) OM4R(48, 100, 20480) OVW(5) OM4R(64, 104, 24576) OVW(4) OM4R(80, 108, 28672)
      "s_waitcnt vmcnt(3) lgkmcnt(15)\n\t" OM4(32, 112)
      OVW(2) OM4(48, 116)
      "s_waitcnt vmcnt(1) lgkmcnt(8)\n\t" OM4(64, 120)
      "s_mov_b64 exec, 1\n\tds_add_rtn_u32 v10, %[ctl], %[fo]\n\ts_mov_b64 exec, -1\n\t" OXR
      "s_waitcnt vmcnt(0) lgkmcnt(5)\n\t" OM4(80, 124)
      OLW(0)
      "v_readfirstlane_b32 %[gt], v10\n\t"
      "s_cmp_eq_u32 %[gt], 0\n\t"
      "s_cbranch_scc1 OH%=\n\t"
      "OS%=:\n\t"
      "s_cmp_ge_u32 %[gt], 0x100\n\t"
      "s_cbranch_scc0 OW%=\n\t"
      "OA%=:\n\t"
      OAD(16) OAD(18) OAD(20) OAD(22) "s_nop 0\n\t" OAD(24) OAD(26) OAD(28) OAD(30)
      OCV(16, 16) OCV(17, 18) OCV(18, 20) OCV(19, 22) OCV(20, 24) OCV(21, 26) OCV(22, 28) OCV(23, 30)
      "v_permlane16_swap_b32 v16, v18\n\tv_permlane16_swap_b32 v17, v19\n\tv_permlane16_swap_b32 v20, v22\n\tv_permlane16_swap_b32 v21, v23\n\t"
      "global_store_dwordx4 %[oso], v[16:19], %[out]\n\t"
      "global_store_dwordx4 %[oso], v[20:23], %[out] offset:64\n\t"
      "s_branch OE%=\n\t"
      "OW%=:\n\t"
      "ds_read_b32 v10, %[ctl]\n\t" OXR
      "s_add_u32 %[spin], %[spin], 1\n\t"
      "s_cmp_gt_u32 %[spin], 0x80000\n\t"
      "s_waitcnt lgkmcnt(0)\n\t"
      "s_cbranch_scc1 OA%=\n\t"
      "v_readfirstlane_b32 %[gt], v10\n\t"
      "s_branch OS%=\n\t"
      "OH%=:\n\t"
      "v_mov_b32 v10, 0x101\n\t"
      "s_nop 4\n\t"
      "ds_write_b128 %[xa], v[16:19]\n\tds_write_b128 %[xa], v[20:23] offset:1024\n\tds_write_b128 %[xa], v[24:27] offset:2048\n\tds_write_b128 %[xa], v[28:31] offset:3072\n\t"
      "ds_write_b32 %[ctl], v10\n\t"
      "s_waitcnt lgkmcnt(0)\n\t"
      "OE%=:\n\t.Lcode_end:\n\t"
      : [w0] "+{v3}"(w0), [w1] "=&{v6}"(w1), [w2] "=&{v7}"(w2), [w3] "=&{v8}"(w3), [xa] "+{v4}"(lv), [fo] "+{v2}"(fo), [ctl] "=&{v9}"(ctl), [gt] "=&s"(gt), [spin] "+s"(spin)
      : [vo] "{v1}"(vo), [oso] "{v5}"(oso), [tag] "s"(tag), [xb] "s"(xb), [cb] "s"(cb), [a0] "s"(ab), [a1] "s"(ab + 8192), [c0] "s"(ab + 32768),
        [c1] "s"(ab + 40960), [fb] "s"(fb), [out] "s"(out)
      : "memory", "scc", "vcc", CL10(1), CL10(2), CL10(3), CL10(4), CL10(5), CL10(6), CL10(7), CL10(8), CL10(9), CL10(10), CL10(11),
        CL4(120, 121, 122, 123), CL4(124, 125, 126, 127));
}

#define G3 3
#define GB 16
#define LA 4
#define PP1 SP(0)
#define SWP 2
#define ABAR 0xAA

__global__ __launch_bounds__(1024, 4) void k_fused(const u16* x_, const u16* wq, const u16* wk, const u16* wv, const u16* gw, char* ws_,
                                                   const u16* __restrict__ wqkv, const u16* __restrict__ woa,
                                                   const u16* __restrict__ wob, u16* __restrict__ out) {
  u32x8 ka; u32x4 kb;
  asm volatile("s_load_dwordx8 %0, %2, 0\n\ts_load_dwordx4 %1, %2, 0x20" : "=&{s[36:43]}"(ka), "=&{s[44:47]}"(kb) : "s"(__builtin_amdgcn_kernarg_segment_ptr()));
  __builtin_amdgcn_sched_barrier(0);
  __shared__ __attribute__((aligned(1024))) char smem[163840];
  const unsigned sb = (unsigned)(size_t)(LDS_AS char*)smem;
  lds_cptr S = (lds_cptr)smem;
  lds_ptr SW = (lds_ptr)smem;
  const int b = blockIdx.x, xcd = b & 7, jj = b >> 3;
  const int tb = 8 * (xcd >> 1) + (jj >> 2), hg = 4 * (xcd & 1) + (jj & 3);
  const int T0 = 64 * tb;
  const int rot = (tb + 2 * (jj & 3)) & 7;
  const int w = __builtin_amdgcn_readfirstlane(threadIdx.x >> 6);
#define LANE_VARS int l = threadIdx.x; asm volatile("" : "+v"(l)); l &= 63; \
  const int g = l >> 4, t = l & 15, q4 = t >> 2, p4 = l & 3, fsw = ((q4 >> 1) & 1) | ((g & 1) << 1)
#define WVO (((l & 7) ^ ((g & 1) << 1)) * 16)
#define RD_TAG(ep) (((u64)(unsigned)__builtin_amdgcn_readfirstlane(ep[1]) << 32) | (unsigned)__builtin_amdgcn_readfirstlane(ep[0]))

  const int q = hg >> 1;
  const int isw = w >> 3;
  const unsigned stride = 128 << (9 * isw);
  unsigned so = rot * stride, dst, pb; u64 pc;
#define PVO(W) (((l >> 3) << (11 - (W))) + ((((l & 7) ^ (((l >> 4) << (2 * (W))) & 7)) * 16) ^ ((w & 1) * 64)) + pb)
  {
    const int l = threadIdx.x & 63;
    unsigned t0, t1, t2, sv;
    asm volatile("s_cmp_lg_u32 %[w], 1\n\ts_cbranch_scc1 PS%=\n\t"
                 "s_getpc_b64 %[pc]\n\tPF%=:\n\ts_mov_b32 %[sv], m0\n\ts_mov_b32 m0, %[snk]\n\t"
                 "v_lshlrev_b32 %[t0], 7, %[l]\n\tv_add_u32 %[t1], 0x2000, %[t0]\n\tv_min_u32 %[t1], .Lcode_end-PF%=, %[t1]\n\t"
                 "global_load_lds_dword %[t0], %[pc]\n\tglobal_load_lds_dword %[t1], %[pc]\n\ts_mov_b32 m0, %[sv]\n\tPS%=:"
                 : [t0] "=&v"(t0), [t1] "=&v"(t1), [pc] "=&s"(pc), [sv] "=&s"(sv) : [l] "v"(l), [w] "s"(w), [snk] "s"(sb + L_PF) : "scc", "memory");
    const int p = (2 * q + w) & 7;
    const unsigned bx = (T0 + 16 * (p & 3) + (p >> 2)) * 1024, bw = p * 8192 + 128 * hg;
    pb = bx + isw * (bw - bx);
    const unsigned vo = PVO(isw);
    const unsigned vk = (8 * ((l >> 3) & 3) + 4 * (l >> 5) + ((l >> 1) & 3)) * 32 + (l & 1) * 16;
    unsigned dw = sb + L_R0;
    dst = sb + isw * L_WQ + p * 1024;
#define PLD(P) "v_add_u32 %[t0], %[so], %[vo]\n\ts_add_u32 %[so], %[so], %[st]\n\ts_and_b32 %[so], %[so], %[mk]\n\t" \
  "global_load_lds_dwordx4 %[t0], %[pc]\n\ts_add_u32 m0, m0, 0x2000\n\t" P
    asm volatile("s_mov_b32 %[sv], m0\n\ts_setprio 3\n\t"
                 "s_cmp_lg_u32 %[w], 0\n\ts_cbranch_scc1 PL%=\n\t"
                 "s_mov_b32 m0, %[lep]\n\tv_lshlrev_b32 %[t0], 2, %[l]\n\tv_xor_b32 %[t2], 64, %[vo]\n\tv_add_u32 %[t2], 0x3c00, %[t2]\n\t"
                 "s_bitcmp1_b32 %[hg], 0\n\ts_waitcnt lgkmcnt(0)\n\ts_cselect_b64 %[pc], s[42:43], s[40:41]\n\t"
                 "global_load_lds_dword %[t0], s[46:47] sc1\n\t"
                 "PK%=:\n\t"
                 "s_mov_b32 m0, %[dw]\n\tv_lshl_add_u32 %[t0], %[so], 4, %[vk]\n\t"
                 "global_load_lds_dwordx4 %[t0], %[pc]\n\tglobal_load_lds_dwordx4 %[t0], %[pc] offset:1024\n\t"
                 "s_mov_b32 m0, %[dst]\n\tv_add_u32 %[t0], %[so], %[vo]\n\tv_add_u32 %[t1], %[so], %[t2]\n\t"
                 "global_load_lds_dwordx4 %[t0], s[36:37]\n\tglobal_load_lds_dwordx4 %[t1], s[36:37] offset:1024\n\t"
                 "s_add_u32 %[so], %[so], %[st]\n\ts_and_b32 %[so], %[so], %[mk]\n\t"
                 "s_add_u32 %[dst], %[dst], 0x2000\n\ts_add_u32 %[dw], %[dw], 0x800\n\t"
                 "s_bitcmp0_b32 %[dw], 15\n\ts_cbranch_scc1 PK%=\n\t"
                 "s_barrier\n\ts_branch PE%=\n\t"
                 "PL%=:\n\t"
                 "s_cmp_lg_u32 %[w], 1\n\ts_cbranch_scc1 PM%=\n\t"
                 "s_mov_b32 m0, %[lgw]\n\tv_lshl_add_u32 %[t0], %[l], 4, %[so]\n\tv_and_b32 %[t0], 0x3ff, %[t0]\n\t"
                 "s_waitcnt lgkmcnt(0)\n\tglobal_load_lds_dwordx4 %[t0], s[44:45]\n\t"
                 "s_barrier\n\ts_branch PE%=\n\t"
                 "PM%=:\n\t"
                 "s_mov_b32 m0, %[dst]\n\ts_bitcmp1_b32 %[w], 3\n\ts_waitcnt lgkmcnt(0)\n\ts_cselect_b64 %[pc], s[38:39], s[36:37]\n\t"
                 "s_barrier\n\t" PLD("") PLD(PP1) PLD("") PLD("")
                 "PE%=:\n\ts_mov_b32 m0, %[sv]"
                 : [t0] "=&v"(t0), [t1] "=&v"(t1), [t2] "=&v"(t2), [sv] "=&s"(sv), [pc] "=&s"(pc), [so] "+s"(so), [dst] "+s"(dst), [dw] "+s"(dw)
                 : "{s[36:43]}"(ka), "{s[44:47]}"(kb), [l] "v"(l), [vo] "v"(vo), [vk] "v"(vk), [w] "s"(w), [st] "s"(stride), [mk] "s"(8 * stride - 1), [hg] "s"(hg),
                   [lep] "s"(sb + L_EP), [lgw] "s"(sb + L_GW)
                 : "scc", "memory");
    asm volatile("" : "+{s[36:43]}"(ka), "+{s[44:47]}"(kb));
  }
  auto more = [&](unsigned vo) {
    unsigned t0, sv;
    asm volatile("s_mov_b32 %[sv], m0\n\ts_add_u32 m0, %[dst], 0x8000\n\t" PLD("") PLD("") PLD("") PLD("") "s_mov_b32 m0, %[sv]"
                 : [t0] "=&v"(t0), [sv] "=&s"(sv), [so] "+s"(so) : [vo] "v"(vo), [st] "s"(stride), [mk] "s"(8 * stride - 1), [pc] "s"(pc), [dst] "s"(dst) : "scc", "memory");
  };
#define WBAR(N) asm volatile("s_waitcnt vmcnt(" STR(N) ") lgkmcnt(0)\n\ts_barrier" ::: "memory")
  const char* x = (const char*)(((u64)ka[1] << 32) | ka[0]);
  char* ws = (char*)(((u64)kb[3] << 32) | kb[2]);

  if (w >= 8) {
    LANE_VARS;
    const int hd = 2 * (w & 1) + ((w >> 2) & 1), tt = (w >> 1) & 1, n = l & 31;
    __builtin_amdgcn_s_setprio(2);
    int ta = L_TAG;
    const unsigned ones = n == 16 ? 0x3f803f80u : 0u;
    const int par = (w >> 2) & 1;
    char* ob_ = ws + WS_O + ((2 * tb + tt) * 64 + 32 + 4 * hg + hd) * 1024 - sb - par * 2048;
    unsigned ga = sb + L_GT + t * 16 + (2 * tt + (g & 1)) * 4, qa = sb + L_QX + (w - 8) * 1024 + l * 16;
    const char* fa = ws + WS_FB + (2 * tb + (hg >> 2)) * 1024 + (hg & 3) * FSP;
    AttRegs ar;
    u32x2 ep;
    att_init(ar, ones, sb + l * 16 + par * 2048);
    asm volatile("" : "+v"(ta), "+s"(ob_), "+v"(ga), "+s"(fa), "+v"(qa));
    if (w < 12) {
      int wa = L_WQ + (8 * (g >> 1) + q4) * 128 + (((hd + (g & 1)) ^ ((q4 ^ g) & 2)) * 32) + (p4 & 1) * 16 + (p4 >> 1) * 8;
      int xa[4];
#pragma unroll
      for (int i = 0; i < 4; ++i) xa[i] = ((32 * tt + n) * 128 + (((l >> 5) ^ ((l >> 1) & 7)) * 16)) ^ (32 * i);
      f32x16 aq = {};
      asm volatile("" : "+v"(wa), "+v"(xa[0]), "+v"(xa[1]), "+v"(xa[2]), "+v"(xa[3]), "+v"(aq));
      bf16x8 wf[32], xf[32];
#pragma unroll
      for (int i = 0; i < 32 + LA; ++i) {
        if (i == 0) { WBAR(2); ep = *(const LDS_AS u32x2*)(S + ta); }
        if (i == 8) { asm volatile("s_waitcnt vmcnt(0)\n\ts_barrier" : "+v"(ep) :: "memory"); }
        if (i == 16 || i == 16 + 4 * G3) { BAR(); }
        if (i < 32) { wf[i] = tr_frag(S + wa + i * 2048, 512); xf[i] = lds_b128(S + xa[i & 3] + (i >> 2) * 8192); }
        __builtin_amdgcn_sched_barrier(0);
        if (i >= LA) aq = MFMA32(wf[i - LA], xf[i - LA], aq);
        __builtin_amdgcn_sched_barrier(0);
      }
      asm volatile("ds_write_b128 %0, %1\n\tds_write_b128 %0, %2 offset:4096" :: "v"(qa),
                   "v"(u32x4{pack2(aq[0], aq[1]), pack2(aq[2], aq[3]), pack2(aq[4], aq[5]), pack2(aq[6], aq[7])}),
                   "v"(u32x4{pack2(aq[8], aq[9]), pack2(aq[10], aq[11]), pack2(aq[12], aq[13]), pack2(aq[14], aq[15])}) : "memory");
    } else {
      unsigned vo = PVO(1), t0, t1, sv;
      asm volatile("s_mov_b32 %[sv], m0\n\ts_add_u32 m0, %[dst], 0x8000\n\t.rept 4\n\t"
                   "v_add_u32 %[t0], %[so], %[vo]\n\ts_add_u32 %[so], %[so], 0x10000\n\tv_xor_b32 %[t1], 0x8000, %[t0]\n\t"
                   "global_load_lds_dwordx4 %[t0], %[pc]\n\ts_xor_b32 m0, m0, 0x1000\n\ts_and_b32 %[so], %[so], 0x7ffff\n\t"
                   "global_load_lds_dwordx4 %[t1], %[pc]\n\ts_xor_b32 m0, m0, 0x1000\n\ts_add_u32 m0, m0, 0x2000\n\t.endr\n\ts_mov_b32 m0, %[sv]"
                   : [t0] "=&v"(t0), [t1] "=&v"(t1), [sv] "=&s"(sv), [so] "+s"(so) : [vo] "v"(vo), [pc] "s"(pc), [dst] "s"(dst) : "scc", "memory");

      WBAR(10);

      ep = *(const LDS_AS u32x2*)(S + ta);
      asm volatile("s_waitcnt vmcnt(8) lgkmcnt(0)\n\ts_barrier" : "+v"(ep) :: "memory");

      WBAR(8-2*G3);

      WBAR(0);

    }
    u64 tag = RD_TAG(ep);
    asm volatile("" : "+s"(tag));
    u32x4 qf;
    f32x16& oo = ar.oo;
    float gate;
    auto fin = [&]() {
      const float f = __builtin_amdgcn_rcpf(oo[8]) * gate;
      return u32x4{pack2(oo[0] * f, oo[1] * f), pack2(oo[2] * f, oo[3] * f), pack2(oo[4] * f, oo[5] * f), pack2(oo[6] * f, oo[7] * f)};
    };

    const unsigned xo = L_R0 - 16384 + (w & 3) * 3072 - par * 2048;
    att_loop(qf, L_QX + (w - 8) * 1024 - par * 2048, xo + par * 20480, xo + 20480 - par * 20480, ar, gate, ga);

    const float big = fmx(fmx(fmx(fmx(fabsf(oo[0]), fabsf(oo[1])), fabsf(oo[2])), fmx(fmx(fabsf(oo[3]), fabsf(oo[4])), fabsf(oo[5]))),
                          fmx(fmx(fabsf(oo[6]), fabsf(oo[7])), oo[8] * 134217728.f));
    u32x4 ob = fin();
    asm volatile("" : "+v"(ob));
    if (__builtin_expect(__builtin_amdgcn_ballot_w64(!(big < 1.7e38f)) != 0, 0)) {
      const bf16x8 qq = __builtin_bit_cast(bf16x8, qf);
      const int l = (ar.ad[3] >> 4) & 63, n = l & 31;
      float m = -__builtin_inff();
#pragma unroll 1
      for (int kt = 0; kt < 64; ++kt) {
        bf16x8 kf;
        asm volatile("global_load_dwordx4 %0, %1, %2 sc1\n\ts_waitcnt vmcnt(0)" : "=&v"(kf) : "v"(WS_KV + kt * 2048 + l * 16), "s"(ws) : "memory");
        m = fmx(m, max16(MFMA32(kf, qq, f32x16{0})));
      }
      m = xmax32(m);
      const unsigned on2 = ((l & 27) == 16) ? 0x3f803f80u : 0u;
      const u32x4 one4 = {on2, on2, on2, on2};
      oo = f32x16{0};
#pragma unroll 1
      for (int kt = 0; kt < 64; ++kt) {
        bf16x8 kf; u32x4 v0, v1;
        asm volatile("global_load_dwordx4 %0, %3, %4 sc1\n\tglobal_load_dwordx4 %1, %3, %4 offset:1024 sc1\n\t"
                     "global_load_dwordx4 %2, %3, %4 offset:1280 sc1\n\ts_waitcnt vmcnt(0)"
                     : "=&v"(kf), "=&v"(v0), "=&v"(v1) : "v"(WS_KV + kt * 2048 + l * 16), "s"(ws) : "memory");
        f32x16 s = MFMA32(kf, qq, f32x16{0});
        f32x8 p0, p1;
#pragma unroll
        for (int i = 0; i < 8; ++i) { p0[i] = __builtin_amdgcn_exp2f(s[i] - m); p1[i] = __builtin_amdgcn_exp2f(s[8 + i] - m); }
        if (n >= 16) { v0 = one4; v1 = one4; }
        oo = __builtin_amdgcn_smfmac_f32_32x32x32_bf16(__builtin_bit_cast(bf16x8, v0), __builtin_bit_cast(bf16x16, p0), oo, 0xdddd, 0, 0);
        oo = __builtin_amdgcn_smfmac_f32_32x32x32_bf16(__builtin_bit_cast(bf16x8, v1), __builtin_bit_cast(bf16x16, p1), oo, 0xdddd, 0, 0);
      }
      ob = fin();
    }
    gst16_sc1(ar.ad[3], ob_, ob);
    if (arrive(sb + L_R0 + 16, 0) == 7) { put_flag(fa, tag); }

    return;
  }

  u64 tag = 0;
  const int tbo = 4 * xcd + (jj >> 3), cg = jj & 7;
  const int to = (w >> 1) & 1, hf = w & 1, kh = w >> 2, pr = w & 3;
  unsigned pvo, pfo, pwo, plv, pso;
#define OUT_VARS pvo = (g >> 1) * 1024 + ((g & 1) * 32 + 16 * hf + t) * 16; pfo = p4 * FSP; plv = l * 16; \
  pwo = sb + (8 * g + q4) * 128 + fsw * 32 + p4 * 8 + kh * 32768; \
  pso = (64 * tbo + 2 * (16 * hf + t) + to) * 1024 + 128 * cg + 32 * (g & 1) + 16 * (g >> 1); \
  asm volatile("" : "+{v1}"(pvo), "+{v2}"(pfo), "+{v3}"(pwo), "+{v4}"(plv), "+{v5}"(pso))
  if (w < 4) {
    __builtin_amdgcn_s_setprio(3);
    LANE_VARS;
    const int kt = 2 * tb + (q >> 1);
    const int tp = (t & 3) | ((t & 4) << 1) | ((t & 8) >> 1);
    unsigned ko = kt * 2048 + (g >> 1) * 512 + (q & 1) * 256 + (g & 1) * 8 + ((hg & 1) ? 1024 + tp * 16 : t * 16);
    OUT_VARS;
    unsigned fo = ((l >> 2) & 1) * 256 + p4 * 8, gto = WS_G + (T0 + l) * 4, gtl = sb + L_GT + l * 4;
    unsigned o0 = (l >> 3) * 1024 + WVO, o1 = (o0 ^ 64) + 8192, o2 = o0 + 16384, o3 = o1 + 16384, zero = 0, cz = sb + L_R0 + 4 * w;
    unsigned vo = PVO(0);
    asm volatile("" : "+v"(fo), "+v"(gto), "+v"(gtl), "+v"(o0), "+v"(o1), "+v"(o2), "+v"(o3), "+v"(zero), "+v"(cz), "+v"(vo));
    int xa = (16 * q + t) * 128 + ((g ^ ((t >> 1) & 7)) * 16), xb = xa ^ 64;
    if (w == 0) {
      int wp = L_R0 + l * 8;
      asm volatile("" : "+v"(xa), "+v"(xb), "+v"(wp), "+v"(ko));
      f32x4 d[2] = {};
      auto proj = [&](auto odd) {
        bf16x8 wf[16], xf[16];
        u32x2 ep;
#pragma unroll
        for (int i = 0; i < 10; ++i) {
          if (i < 8) {
            asm volatile("s_waitcnt vmcnt(%0)" :: "n"(28 - 4 * i) : "memory");
            if (i == 7) { }
            if (i == 0) ep = *(const LDS_AS u32x2*)(S + L_EP);
#pragma unroll
            for (int j = 2 * i; j < 2 * i + 2; ++j) { wf[j] = tr_frag(S + wp + j * 1024, 512); xf[j] = lds_b128(S + ((j & 1) ? xb : xa) + i * 8192); }
          }
          __builtin_amdgcn_sched_barrier(0);
          if (i >= 2) {
#pragma unroll
            for (int j = 2 * i - 4; j < 2 * i - 2; ++j) d[j & 1] = odd ? MFMA16(xf[j], wf[j], d[j & 1]) : MFMA16(wf[j], xf[j], d[j & 1]);
          }
          if (i == 2) { tag = RD_TAG(ep) * 6364136223846793005ull + 1442695040888963407ull; asm volatile("" : "+s"(tag)); }
          __builtin_amdgcn_sched_barrier(0);
        }
      };
      if (hg & 1) proj(std::true_type{}); else proj(std::false_type{});
      const u32x2 kv = __builtin_bit_cast(u32x2, pack4((d[0] + d[1]) * ((hg & 1) ? 1.f : QSCALE)));
      gst16_sc1(2 * ko, ws + WS_KG, u32x4{kv[0], (unsigned)tag, kv[1], (unsigned)tag});
      gst8_sc1(ko, ws + WS_KV, kv);
      *(LDS_AS u64*)(SW + L_TAG) = tag;
    }

    if (w == 1 && (hg & 1)) {
      int ga = L_GW + g * 16;
      asm volatile("s_waitcnt vmcnt(0)" : "+v"(ga), "+v"(xa), "+v"(xb) :: "memory");
      bf16x8 gf[16], xf[16];
#pragma unroll
      for (int j = 0; j < 16; ++j) gf[j] = lds_b128(S + ga + j * 64);
      asm volatile("s_waitcnt lgkmcnt(0)\n\ts_barrier" : "+v"(gf[0]), "+v"(gf[1]), "+v"(gf[2]), "+v"(gf[3]), "+v"(gf[4]), "+v"(gf[5]), "+v"(gf[6]), "+v"(gf[7]),
                   "+v"(gf[8]), "+v"(gf[9]), "+v"(gf[10]), "+v"(gf[11]), "+v"(gf[12]), "+v"(gf[13]), "+v"(gf[14]), "+v"(gf[15]) :: "memory");

      const u32x2 ep = *(const LDS_AS u32x2*)(S + L_TAG);
      f32x4 d[2] = {};
#pragma unroll
      for (int j = 0; j < 24; ++j) {
        if (j == GB) { tag = RD_TAG(ep); asm volatile("s_barrier" : "+s"(tag) :: "memory"); }
        if (j < 16) xf[j] = lds_b128(S + ((j & 1) ? xb : xa) + (j >> 1) * 8192);
        __builtin_amdgcn_sched_barrier(0);
        if (j >= 8) d[j & 1] = MFMA16(gf[j - 8], xf[j - 8], d[j & 1]);
        __builtin_amdgcn_sched_barrier(0);
      }
      const float dz = d[0][0] + d[1][0];
      if (l < 16) gst4_sc1(WS_G + (T0 + 4 * l + q) * 4, ws, __float_as_uint(__builtin_amdgcn_rcpf(1.f + __expf(-dz))));
    } else {
      if (w >= 2) more(vo);
      WBAR(6);

      const u32x2 ep = *(const LDS_AS u32x2*)(S + L_TAG);
      tag = RD_TAG(ep);
      WBAR(4);
    }

    int ct = 16 * (xcd >> 1), slot = L_R0;
    {
      const unsigned go = w * 8192 + l * 16;
      const unsigned la0 = sb + w * 4096 + l * 8;
      f32x32 a;
#define GLD(R, O, P) "global_load_dwordx4 v[" STR(R) ":" STR(R) "+3], %[go], " P " offset:" STR(O) " sc1\n\t"
#define SWEEP(P) GLD(96, -4096, P) GLD(100, -3072, P) GLD(104, -2048, P) GLD(108, -1024, P) GLD(112, 0, P) GLD(116, 1024, P) GLD(120, 2048, P) GLD(124, 3072, P)
#define GCK(D, R) "v_bitop3_b32 " D ", v[" STR(R) "+1], v[" STR(R) "+3], %[tag] bitop3:0x7e\n\t"
#define GW2(A, R) "ds_write2_b32 " A ", v[" STR(R) "], v[" STR(R) "+2] offset1:1\n\tds_write2_b32 " A ", v[" STR(R) "+4], v[" STR(R) "+6] offset0:128 offset1:129\n\t"
      const char* gp = ws + WS_KG + 4096 + ct * 4096;
#define SWA(N) asm volatile(SWEEP("%[gp]") "s_waitcnt vmcnt(" STR(N) "+8)\n\ts_barrier" : "={v[96:127]}"(a) : [go] "v"(go), [gp] "s"(gp) : "memory")
#define SWB(N) asm volatile("s_waitcnt vmcnt(" STR(N) ")\n\ts_barrier" : "+{v[96:127]}"(a) :: "memory")
      WBAR(4-G3);
      SWA(0);

#pragma unroll 1
      for (int c = 0; c < 2; ++c) {
        const char* gn = gp + 32768;
        unsigned t0, t1, t2, t3, it = 0;
        asm volatile(
            "GS%=:\n\ts_waitcnt vmcnt(0)\n\t"
            GCK("%[t0]", 96) GCK("%[t1]", 100) GCK("%[t2]", 104) GCK("%[t3]", 108) "v_or3_b32 %[t0], %[t0], %[t1], %[t2]\n\t"
            GCK("%[t1]", 112) GCK("%[t2]", 116) "v_or3_b32 %[t0], %[t0], %[t3], %[t1]\n\t"
            GCK("%[t3]", 120) GCK("%[t1]", 124) "v_or3_b32 %[t0], %[t0], %[t2], %[t3]\n\tv_or_b32 %[t0], %[t0], %[t1]\n\t"
            "v_cmp_ne_u32 vcc, 0, %[t0]\n\t"
            "s_add_u32 %[it], %[it], 1\n\t"
            "s_cbranch_vccz GW%=\n\t"
            "s_cmp_gt_u32 %[it], 0x40000\n\t"
            "s_cbranch_scc1 GW%=\n\t"
            SWEEP("%[gp]")
            "s_branch GS%=\n\t"
            "GW%=:\n\t"
            "v_add_u32 %[t0], %[slot], %[la]\n\tv_add_u32 %[t1], 0x400, %[t0]\n\tv_add_u32 %[t2], 0x800, %[t0]\n\tv_add_u32 %[t3], 0xc00, %[t0]\n\t"
            GW2("%[t0]", 96) GW2("%[t1]", 104) GW2("%[t2]", 112) GW2("%[t3]", 120)
            "s_cmp_eq_u32 %[c], 1\n\t"
            "s_cbranch_scc1 GE%=\n\t"
            SWEEP("%[gn]")
            "GE%=:"
            : "+{v[96:127]}"(a), [t0] "=&v"(t0), [t1] "=&v"(t1), [t2] "=&v"(t2), [t3] "=&v"(t3), [it] "+s"(it)
            : [go] "v"(go), [gp] "s"(gp), [gn] "s"(gn), [tag] "s"((unsigned)tag), [la] "v"(la0), [slot] "s"(slot), [c] "s"(c) : "memory", "scc", "vcc");

        gp = gn; slot -= 16384;
        if (c == 1 && w == 0) put_flag(ws + WS_FKV + kt * 256 + (hg & 3) * 8, tag);
        LBAR();

      }
      ct = (ct + 16) & 63;
    }
#pragma unroll 1
    for (int c = 2; c < 8; ++c) {
      poll_flags(fo, ws + WS_FKV + (ct + 2 * w) * 256, tag);

      const char* kp = ws + WS_KV + (ct + 2 * w) * 2048;
      u32x4 a0 = gld16_sc1<0>(plv, kp), a1 = gld16_sc1<1024>(plv, kp), a2 = gld16_sc1<2048>(plv, kp), a3 = gld16_sc1<3072>(plv, kp);
      asm volatile("s_waitcnt vmcnt(0)" : "+v"(a0), "+v"(a1), "+v"(a2), "+v"(a3) :: "memory");
      lds_ptr dp = SW + (slot + w * 4096) + plv;
      *(LDS_AS u32x4*)dp = a0; *(LDS_AS u32x4*)(dp + 1024) = a1; *(LDS_AS u32x4*)(dp + 2048) = a2; *(LDS_AS u32x4*)(dp + 3072) = a3;
      if (c == 3 && w == 0) {
        poll_flags(fo, ws + WS_FKV + tb * 512, tag);
        asm volatile("global_load_dword %0, %0, %2 sc1\n\ts_waitcnt vmcnt(0)\n\tds_write_b32 %1, %0" : "+v"(gto) : "v"(gtl), "s"(ws) : "memory");
      }
      if (c >= 6) {
        const char* p = (const char*)(c == 6 ? woa : wob) + 128 * cg + w * 32768;
        const unsigned dst = sb + (c & 1) * 65536 + w * 4096;
#pragma unroll
        for (int i = 0; i < 4; ++i)
          if (i < 3 || c == 6) dma4(o0, o1, o2, o3, p + i * 131072, dst + i * 16384);
      }
      ct = (ct + 8) & 63; slot = slot == L_S2 ? L_R0 : slot - 16384;

      LBAR();

    }

    dma4(o0, o1, o2, o3, (const char*)wob + 128 * cg + w * 32768 + 393216, sb + L_S2 + w * 4096);
    asm volatile("ds_write2_b32 %0, %1, %1 offset1:4\n\ts_waitcnt lgkmcnt(0)" :: "v"(cz), "v"(zero) : "memory");
    __builtin_amdgcn_s_setprio(0);
    if (b == 0 && w == 0) put_flag(ws + WS_EPOCH, tag);
  } else {
    LANE_VARS;
    const int hd = w & 3;
    unsigned wvo = (8 * ((l >> 3) & 3) + 4 * (l >> 5) + ((l >> 1) & 3)) * 3072 + (l & 1) * 16;
    const char* wsrc = (const char*)wqkv + (4 * hg + hd) * 32;
    const int ring = L_WQ + hd * 6144;
    const int th = T0 - 16 + 2 * (t & 7);
    unsigned hvo = ((th < 0 ? 0 : th) + (t >> 3)) * 1024 + g * 16;
    int ks = 2 * rot;
    bf16x8 hx[2] = {};
    auto stage_w = [&](int sl) {
      dma3(wvo, wsrc + ks * 98304, sb + ring + sl * 3072);
      asm volatile("global_load_dwordx4 %0, %1, %2" : "+v"(hx[sl]) : "v"(hvo), "s"((const char*)x + ks * 64) : "memory");
      ks = (ks + 1) & 15;
    };
    int xa = t * 128 + ((g ^ ((t >> 1) & 7)) * 16);
    f32x4 aa[4] = {}, ak[5] = {}, av[5] = {};
    f32x4 mo, mp, mh;
#pragma unroll
    for (int r = 0; r < 4; ++r) {
      mo[r] = ((unsigned)(t - 4 * g - r) <= 7u) ? 0.f : -__builtin_inff();
      mp[r] = (4 * g + r >= t + 9) ? 0.f : -__builtin_inff();
      mh[r] = (tb > 0 && g < 2 && 4 * g + r > t) ? 0.f : -__builtin_inff();
    }
    int wr = ring + l * 8, gl = L_GT + t * 16;
    unsigned ml[2];
#pragma unroll
    for (int r = 0; r < 2; ++r) ml[r] = (4 * g + 2 * r <= t ? 0xffffu : 0u) | (4 * g + 2 * r + 1 <= t ? 0xffff0000u : 0u);
    OUT_VARS;
    unsigned vo = PVO(0);
    asm volatile("" : "+v"(gl), "+v"(ml[0]), "+v"(ml[1]), "+v"(vo));
    asm volatile("" : "+v"(aa[0]), "+v"(aa[1]), "+v"(aa[2]), "+v"(aa[3]), "+v"(ak[0]), "+v"(ak[1]), "+v"(ak[2]), "+v"(ak[3]), "+v"(ak[4]),
                      "+v"(av[0]), "+v"(av[1]), "+v"(av[2]), "+v"(av[3]), "+v"(av[4]), "+v"(mo), "+v"(mp), "+v"(mh),
                      "+v"(hx[0]), "+v"(hx[1]), "+v"(xa), "+v"(wr), "+v"(wvo), "+v"(hvo));

    more(vo);

    WBAR(6);
    {
      const u32x2 ep = *(const LDS_AS u32x2*)(S + L_TAG);
      tag = RD_TAG(ep);
    }
    WBAR(4);
    WBAR(4-G3);
    WAIT_BAR(0);

    BAR();
    stage_w(0); stage_w(1);
    BAR();
#pragma unroll 1
    for (int j = 0; j < 8; ++j) {
#pragma unroll
      for (int h = 0; h < 2; ++h) {
        bf16x8 wf[3], xf[4];
#pragma unroll
        for (int i = 0; i < 4; ++i) xf[i] = lds_b128(S + (xa ^ (h * 64)) + i * 2048);
        asm volatile("s_waitcnt vmcnt(4)" : "+v"(hx[h]) :: "memory");
#pragma unroll
        for (int m = 0; m < 3; ++m) wf[m] = tr_frag(S + wr + h * 3072 + m * 1024, 512);
        asm volatile("s_waitcnt lgkmcnt(0)" : "+v"(wf[0]), "+v"(wf[1]), "+v"(wf[2]) :: "memory");
        ak[4] = MFMA16(wf[1], hx[h], ak[4]); av[4] = MFMA16(hx[h], wf[2], av[4]);
        asm volatile("" : "+v"(ak[4]), "+v"(av[4]));
        stage_w(h);
#pragma unroll
        for (int i = 0; i < 4; ++i) av[i] = MFMA16(xf[i], wf[2], av[i]);
        __builtin_amdgcn_sched_barrier(0);
#pragma unroll
        for (int i = 0; i < 4; ++i) ak[i] = MFMA16(wf[1], xf[i], ak[i]);
        __builtin_amdgcn_sched_barrier(0);
#pragma unroll
        for (int i = 3; i >= 0; --i) aa[i] = MFMA16(wf[0], xf[i], aa[i]);
        __builtin_amdgcn_sched_barrier(0);
      }
      xa += 8192;

      if ((ABAR >> j) & 1) { BAR(); }
    }
    asm volatile("s_waitcnt vmcnt(0)" : "+v"(hx[0]), "+v"(hx[1]) :: "memory");

    const f32x4 gt = *(const LDS_AS f32x4*)(S + gl);
    const s16x4 on = {0x3f80, 0x3f80, 0x3f80, 0x3f80};
    const f32x4 z = {0, 0, 0, 0};
    s16x4 kk[5], vv[5], ob[4];
    f32x4 sc[4];
    float m[4];
#pragma unroll
    for (int i = 0; i < 5; ++i) { kk[i] = pack4(ak[i]); vv[i] = pack4(av[i]); }
#pragma unroll
    for (int j = 0; j < 4; ++j) {
      const int i = j ^ (j >> 1);
      if (i == 2 && tb == 0) { mp = mh; asm volatile("" : "+v"(mp)); }
      const s16x4 q = pack4(aa[i]);
      const f32x4 so = MFMA16K(kk[i], q, mo), sp = MFMA16K(kk[(i & 1) ? i - 1 : 4], q, i ? mp : mh);
#pragma unroll
      for (int r = 0; r < 4; ++r) sc[i][r] = fmx(so[r], sp[r]);
      m[i] = fmx(fmx(fmx(sc[i][0], sc[i][1]), sc[i][2]), sc[i][3]);
    }
    {
      auto r0 = __builtin_amdgcn_permlane16_swap(__float_as_uint(m[0]), __float_as_uint(m[1]), false, false);
      auto r1 = __builtin_amdgcn_permlane16_swap(__float_as_uint(m[2]), __float_as_uint(m[3]), false, false);
      auto r2 = __builtin_amdgcn_permlane32_swap(__float_as_uint(fmx(__uint_as_float(r0[0]), __uint_as_float(r0[1]))),
                                                 __float_as_uint(fmx(__uint_as_float(r1[0]), __uint_as_float(r1[1]))), false, false);
      const unsigned y = __float_as_uint(fmx(__uint_as_float(r2[0]), __uint_as_float(r2[1])) * -QSCALE);
      auto u = __builtin_amdgcn_permlane32_swap(y, y, false, false);
      auto u0 = __builtin_amdgcn_permlane16_swap(u[0], u[0], false, false), u1 = __builtin_amdgcn_permlane16_swap(u[1], u[1], false, false);
      m[0] = __uint_as_float(u0[0]); m[1] = __uint_as_float(u0[1]); m[2] = __uint_as_float(u1[0]); m[3] = __uint_as_float(u1[1]);
    }
#pragma unroll
    for (int i = 0; i < 4; ++i) {
      f32x4 e = __builtin_elementwise_fma(sc[i], f32x4{QSCALE, QSCALE, QSCALE, QSCALE}, f32x4{m[i], m[i], m[i], m[i]});
#pragma unroll
      for (int r = 0; r < 4; ++r) e[r] = __builtin_amdgcn_exp2f(e[r]);
      const s16x4 pk = pack4(e);
      const u32x2 pu = __builtin_bit_cast(u32x2, pk);
      const u32x4 ps = {pu[0] & ~ml[0], pu[1] & ~ml[1], pu[0] & ml[0], pu[1] & ml[1]};
      const s16x8 v2 = __builtin_shufflevector(vv[(i & 1) ? i - 1 : 4], vv[i], 0, 1, 2, 3, 4, 5, 6, 7);
      const f32x4 ls = MFMA16K(on, pk, z);
      const f32x4 o = MFMA16(__builtin_bit_cast(bf16x8, v2), __builtin_bit_cast(bf16x8, ps), z);
      ob[i] = pack4(o * (__builtin_amdgcn_rcpf(ls[0]) * gt[i]));
      if (i & 1) gst16_sc1(plv, ws + WS_O + ((2 * tb + (i >> 1)) * 64 + 4 * hg + hd) * 1024, join16(ob[i - 1], ob[i]));
    }

    unsigned c0 = *(const LDS_AS unsigned*)(S + L_S2);
    asm volatile("s_waitcnt lgkmcnt(0)\n\ts_barrier" : "+v"(c0) :: "memory");

    if (arrive(sb + L_S2, c0) == 3) put_flag(ws + WS_FA + (2 * tb + (hg >> 2)) * 1024 + (hg & 3) * FSP, tag);

    asm volatile("global_store_dwordx4 %0, %1, %2\n\tglobal_store_dwordx4 %0, %1, %2 offset:64\n\ts_nop 1" :: "v"(pso), "v"(mo), "s"(out) : "memory");
    BAR();

  }

  poll_flags(pfo, ws + WS_FA + (2 * tbo + kh) * 1024, tag);

  out_proj(pvo, pfo, pwo, plv, pso, sb + 4096 * pr, sb + L_R0 + 4 * pr, tag,
           ws + WS_O + ((2 * tbo + to) * 64 + 16 * kh) * 1024 + 4096, ws + WS_FB + (2 * tbo + kh) * 1024, out
           );

  __builtin_amdgcn_endpgm();
}

extern "C" void kernel_launch(void* const* d_in, const int* in_sizes, int n_in, void* d_out, int out_size, void* d_ws,
                              size_t ws_size, hipStream_t stream) {
  k_fused<<<256, 1024, 0, stream>>>((const u16*)d_in[0], (const u16*)d_in[4], (const u16*)d_in[5], (const u16*)d_in[6],
                                    (const u16*)d_in[8], (char*)d_ws, (const u16*)d_in[1], (const u16*)d_in[2],
                                    (const u16*)d_in[7], (u16*)d_out);
}
```
